```python
import torch

def _softmax(x, axis=-1):
    return torch.softmax(x, dim=axis)
def _gelu(x):
    return torch.nn.functional.gelu(x, approximate="tanh")
def _silu(x):
    return torch.nn.functional.silu(x)
def _sigmoid(x):
    return torch.sigmoid(x)
def _layernorm(x, axis=-1, eps=1e-5):
    m = x.mean(dim=axis, keepdim=True)
    v = x.var(dim=axis, keepdim=True, unbiased=False)
    return (x - m) / torch.sqrt(v + eps)
def _scatter_add(tgt, idx, src, axis=0):
    return tgt.clone().index_add_(axis, idx, src)
def _segment_sum(src, seg, n):
    out = torch.zeros((n,) + src.shape[1:], dtype=src.dtype)
    return out.index_add_(0, seg, src)
def _pad(x, pw):
    flat = tuple(v for pair in reversed(pw) for v in pair)
    return torch.nn.functional.pad(x, flat)

kernel_name = 'gather_scatter__cheap_equivariant_egnn__graph'

def setup_inputs():

    _w = (lambda a: (lambda t: t.to(torch.bfloat16) if t.is_floating_point() else t)(torch.as_tensor(a)))
    N, deg, D, De = 10000, 32, 128, max(0, 1)
    E = N * deg
    return {
        "h": _w((lambda *s: torch.randn(*s, dtype=torch.bfloat16) * 0.2)(N, D)),
        "pos": _w((lambda *s: torch.randn(*s, dtype=torch.bfloat16) * 0.8)(N, 3)),
        "e_attr": _w((lambda *s: torch.randn(*s, dtype=torch.bfloat16) * 0.8)(E, De)),
        "src": _w((lambda lo, hi, *s: torch.randint(lo, hi, s))(0, N, E).to(torch.int64)),
        "dst": _w((lambda lo, hi, *s: torch.randint(lo, hi, s))(0, N, E).to(torch.int64)),
        "w_e": _w(0.1 * (lambda *s: torch.randn(*s, dtype=torch.bfloat16) * 0.8)(2 * D + 1 + De, D)),
        "w_x": _w(0.1 * (lambda *s: torch.randn(*s, dtype=torch.bfloat16) * 0.2)(D, 1)),
        "w_h": _w(0.1 * (lambda *s: torch.randn(*s, dtype=torch.bfloat16) * 0.2)(2 * D, D)),
    }

def reference(h, pos, e_attr, src, dst, w_e, w_x, w_h):
    N, D = h.shape[0], h.shape[1]
    diff = pos[dst] - pos[src]
    dist2 = torch.sum(diff * diff, axis=-1, keepdims=True)
    e_in = torch.concatenate([h[dst], h[src], dist2, e_attr], axis=-1)
    m_ij = _silu(e_in @ w_e)
    coord_scale = m_ij @ w_x
    pos_new = pos + _segment_sum(diff * coord_scale, dst, N)
    m_i = _segment_sum(m_ij, dst, N)
    h_new = h + _silu(torch.concatenate([h, m_i], axis=-1) @ w_h)
    return h_new, pos_new
```

```cpp
#include <hip/hip_runtime.h>
#include <hip/hip_bf16.h>
#include <stdint.h>

typedef unsigned short u16;
__device__ __forceinline__ float bf2f(u16 x){ return __uint_as_float(((unsigned)x)<<16); }
__device__ __forceinline__ u16 f2bf(float f){ unsigned u=__float_as_uint(f); u += 0x7fffu + ((u>>16)&1u); return (u16)(u>>16); }
__device__ __forceinline__ float rbf(float f){ return bf2f(f2bf(f)); }

__global__ void k_ab(const u16* h, const u16* we, float* A, float* B, float* macc, float* pacc, int N){
  int n = blockIdx.x; int c = threadIdx.x;
  __shared__ float hs[128];
  hs[c] = bf2f(h[n*128+c]);
  __syncthreads();
  float a=0.f,b=0.f;
  for(int k=0;k<128;k++){ a += hs[k]*bf2f(we[k*128+c]); b += hs[k]*bf2f(we[(128+k)*128+c]); }
  A[n*128+c]=a; B[n*128+c]=b; macc[n*128+c]=0.f;
  if(c<4) pacc[n*4+c]=0.f;
}
__global__ void k_edge(const u16* pos, const u16* ea, const int* src, const int* dst, const u16* we, const u16* wx,
                       const float* A, const float* B, float* macc, float* pacc, int E){
  int e = blockIdx.x; int c = threadIdx.x;
  int s = src[e], d = dst[e];
  float dx = rbf(bf2f(pos[d*3])-bf2f(pos[s*3]));
  float dy = rbf(bf2f(pos[d*3+1])-bf2f(pos[s*3+1]));
  float dz = rbf(bf2f(pos[d*3+2])-bf2f(pos[s*3+2]));
  float d2 = rbf(rbf(dx*dx)+rbf(dy*dy)+rbf(dz*dz));
  float pre = A[d*128+c]+B[s*128+c]+d2*bf2f(we[256*128+c])+bf2f(ea[e])*bf2f(we[257*128+c]);
  float m = pre/(1.f+__expf(-pre));
  atomicAdd(&macc[d*128+c], m);
  float p = m*bf2f(wx[c]);
  for(int o=32;o>0;o>>=1) p += __shfl_xor(p,o);
  __shared__ float ps[2];
  if((c&63)==0) ps[c>>6]=p;
  __syncthreads();
  if(c<3){ float cs=ps[0]+ps[1]; float dd = c==0?dx:(c==1?dy:dz); atomicAdd(&pacc[d*4+c], dd*cs); }
}
__global__ void k_node(const u16* h, const u16* pos, const u16* wh, const float* macc, const float* pacc, u16* out, int N){
  int n = blockIdx.x; int c = threadIdx.x;
  __shared__ float xs[256];
  xs[c] = bf2f(h[n*128+c]); xs[128+c]=rbf(macc[n*128+c]);
  __syncthreads();
  float a=0.f;
  for(int k=0;k<256;k++) a += xs[k]*bf2f(wh[k*128+c]);
  float s = a/(1.f+__expf(-a));
  out[n*128+c]=f2bf(xs[c]+s);
  if(c<3) out[N*128+n*3+c]=f2bf(bf2f(pos[n*3+c])+pacc[n*4+c]);
}
extern "C" void kernel_launch(void* const* d_in, const int* in_sizes, int n_in, void* d_out, int out_size, void* d_ws, size_t ws_size, hipStream_t stream){
  const u16* h=(const u16*)d_in[0]; const u16* pos=(const u16*)d_in[1]; const u16* ea=(const u16*)d_in[2];
  const int* src=(const int*)d_in[3]; const int* dst=(const int*)d_in[4];
  const u16* we=(const u16*)d_in[5]; const u16* wx=(const u16*)d_in[6]; const u16* wh=(const u16*)d_in[7];
  int N=in_sizes[0]/128; int E=in_sizes[3];
  float* A=(float*)d_ws; float* B=A+N*128; float* macc=B+N*128; float* pacc=macc+N*128;
  k_ab<<<N,128,0,stream>>>(h,we,A,B,macc,pacc,N);
  k_edge<<<E,128,0,stream>>>(pos,ea,src,dst,we,wx,A,B,macc,pacc,E);
  k_node<<<N,128,0,stream>>>(h,pos,wh,macc,pacc,(u16*)d_out,N);
}
```

```cpp
#include <hip/hip_runtime.h>
#include <stdint.h>

typedef unsigned short u16;
typedef unsigned int u32;
typedef __attribute__((ext_vector_type(8))) short s8;
typedef __attribute__((ext_vector_type(4))) short s4;
typedef __attribute__((ext_vector_type(2))) float f2;
typedef __attribute__((ext_vector_type(4))) float f4;
typedef __attribute__((ext_vector_type(16))) float f16v;
typedef __attribute__((ext_vector_type(2))) __bf16 bf2;
typedef __attribute__((ext_vector_type(2))) u32 u2;
typedef __attribute__((ext_vector_type(4))) u32 u4;
#define LDSP(T, a) ((__attribute__((address_space(3))) T*)(size_t)(a))
#define DI __device__ __forceinline__

constexpr int NN = 10000, EE = 320000;
constexpr int NB = 250, NPB = 40, NG = 250, CH = 1280, CAP1 = 8, CAP2 = 24;
constexpr float NLOG2E = -1.4426950408889634f, NLN2 = -0.6931471805599453f;
constexpr size_t WS_BT = 0;
constexpr size_t WS_WT = 2560256 + 3840;
constexpr size_t WS_S1 = WS_WT + 3 * 32768;
constexpr size_t WS_S2 = WS_S1 + (size_t)NB * NG * CAP1 * 16;
constexpr size_t WS_END = WS_S2 + (size_t)NB * NG * CAP2 * 16;
constexpr int L1_POS = 0, L1_W2 = 60416, L1_H = L1_W2 + 32768, L1_IMG = L1_H + 12288, L1_CNT = L1_IMG + 32000,
              L1_OUT = L1_CNT + 1024, L1_END = L1_OUT + 48 * 272;
constexpr int WCAP = 224, SP = 456, SBLK = 23 * SP;
constexpr int L2_S = 0, L2_FLAG = 8 * SBLK, L2_STG = L2_FLAG + 16, STG = 4160, L2_PARK = L2_STG + 16 * STG,
              L2_END = L2_PARK + 10240;
constexpr int MP = 144, PART = 1024;
static_assert(L2_STG % 16 == 0 && L2_END <= 163840 && L1_END <= 163840, "lds");

#define STAMP(buf, i)

DI float bflo(u32 w) { return __uint_as_float(w << 16); }
DI float bfhi(u32 w) { return __uint_as_float(w & 0xffff0000u); }
DI u32 pk2(float a, float b) { f2 v; v.x = a; v.y = b; return __builtin_bit_cast(u32, __builtin_convertvector(v, bf2)); }
DI __amdgpu_buffer_rsrc_t mkrsrc(const void* p, int bytes) { return __builtin_amdgcn_make_buffer_rsrc((void*)p, 0, bytes, 0x00020000); }
DI void dma16(__amdgpu_buffer_rsrc_t r, u32 lds, u32 voff) { __builtin_amdgcn_raw_ptr_buffer_load_lds(r, LDSP(void, lds), 16, voff, 0, 0, 0); }
DI u4 ld16(__amdgpu_buffer_rsrc_t r, u32 voff) { return __builtin_amdgcn_raw_buffer_load_b128(r, voff, 0, 0); }
DI s8 trfrag(u32 a0, u32 a1) {
  s4 lo = __builtin_amdgcn_ds_read_tr16_b64_v4i16(LDSP(s4, a0));
  s4 hi = __builtin_amdgcn_ds_read_tr16_b64_v4i16(LDSP(s4, a1));
  return __builtin_shufflevector(lo, hi, 0, 1, 2, 3, 4, 5, 6, 7);
}
#define MF16(a, b, c) __builtin_amdgcn_mfma_f32_16x16x32_bf16((a), (b), (c), 0, 0, 0)
#define MF32(a, b, c) __builtin_amdgcn_mfma_f32_32x32x16_bf16((a), (b), (c), 0, 0, 0)

DI void geom(u32 dxy, u32 dz_, u32 sxy, u32 sz_, u32& w2, u32& dz, u32& d2) {
  w2 = pk2(bflo(dxy) - bflo(sxy), bfhi(dxy) - bfhi(sxy));
  dz = pk2(bflo(dz_) - bflo(sz_), 0.f);
  const float x = bflo(w2), y = bfhi(w2), z = bflo(dz);
  const u32 q = pk2(x * x, y * y), qz = pk2(z * z, 0.f);
  d2 = pk2(bflo(q) + bfhi(q) + bflo(qz), 0.f);
}

__global__ __launch_bounds__(1024) void k1(const u16* __restrict__ h, const u16* __restrict__ pos, const u16* __restrict__ ea,
                                           const int* __restrict__ src, const int* __restrict__ dst, const u16* __restrict__ we,
                                           const u16* __restrict__ wh, char* __restrict__ ws) {
  const int g = blockIdx.x, t = threadIdx.x, lane = t & 63, w = __builtin_amdgcn_readfirstlane(t >> 6);
  STAMP(st, 0);
  if (g >= NG) {
    const int mat = g - NG;
    const u16* W = mat == 0 ? we : (mat == 1 ? wh : wh + 128 * 128);
#pragma unroll
    for (int i = 0; i < 2; ++i) {
      const int f = t + 1024 * i, ct = f >> 8, ks = (f >> 6) & 3;
      const u16* p = W + (32 * ks + 8 * (lane >> 4)) * 128 + 16 * ct + (lane & 15);
      u4 v;
#pragma unroll
      for (int j = 0; j < 4; ++j) v[j] = (u32)p[(2 * j) * 128] | ((u32)p[(2 * j + 1) * 128] << 16);
      *(u4*)(ws + WS_WT + mat * 32768 + f * 16) = v;
    }
    if (mat == 0 && t < 16) { u4 z = {0, 0, 0, 0}; *(u4*)(ws + WS_BT + t * 16) = z; }
    return;
  }
  u2 dv = {0, 0}, sv = {0, 0}; u32 ev = 0;
  if (w < 10) {
    const int e0 = g * CH + 2 * t;
    dv = *(const u2*)(dst + e0); sv = *(const u2*)(src + e0); ev = *(const u32*)(ea + e0);
  }
  {
    const __amdgpu_buffer_rsrc_t rp = mkrsrc(pos, NN * 6), rw = mkrsrc(we + 128 * 128, 32768), rh = mkrsrc(h + (size_t)g * NPB * 128, NPB * 256);
#pragma unroll
    for (int i = 0; i < 4; ++i) { const int p = w + 16 * i; if (p < 59) dma16(rp, L1_POS + p * 1024, p * 1024 + lane * 16); }
#pragma unroll
    for (int i = 0; i < 2; ++i) { const int p = w + 16 * i; dma16(rw, L1_W2 + p * 1024, p * 1024 + lane * 16); }
    if (w >= 4) { const int p = w - 4, row = 4 * p + (lane >> 4); dma16(rh, L1_H + p * 1024, row * 256 + (((lane & 15) ^ (row & 15)) << 4)); }
  }
  if (t < 256) *LDSP(u32, L1_CNT + 4 * t) = 0;
  asm volatile("s_waitcnt vmcnt(0)" ::: "memory");
  __syncthreads();
  STAMP(st, 1);
  if (w < 10) {
#pragma unroll
    for (int i = 0; i < 2; ++i) {
      const u32 d = dv[i], s = sv[i];
      const u32 ad = (d * 6) & ~3u, as = (s * 6) & ~3u;
      const u32 d0 = *LDSP(u32, L1_POS + ad), d1 = *LDSP(u32, L1_POS + ad + 4);
      const u32 s0 = *LDSP(u32, L1_POS + as), s1 = *LDSP(u32, L1_POS + as + 4);
      const u32 wg = (d * 52429u) >> 18, b = wg >> 3, nw = d - 5 * wg;
      const u32 rank = __hip_atomic_fetch_add(LDSP(u32, L1_CNT + 4 * b), 1u, __ATOMIC_RELAXED, __HIP_MEMORY_SCOPE_WORKGROUP);
      const u32 shd = (d & 1) << 4, shs = (s & 1) << 4;
      u32 w2, dz, d2;
      geom(__builtin_amdgcn_alignbit(d1, d0, shd), d1 >> shd, __builtin_amdgcn_alignbit(s1, s0, shs), s1 >> shs, w2, dz, d2);
      u4 e;
      e[0] = (s + 1) | (nw << 29);
      e[1] = (d2 & 0xffffu) | (i == 0 ? (ev << 16) : (ev & 0xffff0000u));
      e[2] = w2;
      e[3] = (dz & 0xffffu) | (((wg & 7) * (SBLK / 8)) << 16);
      if (rank < CAP1) *LDSP(u4, L1_IMG + (b * CAP1 + rank) * 16) = e;
      else if (rank < CAP1 + CAP2) *(u4*)(ws + WS_S2 + ((size_t)(b * NG + g) * CAP2 + (rank - CAP1)) * 16) = e;
    }
  } else {
    const int i = w - 10, rt = i % 3, cg = i / 3, c = lane & 15, q = lane >> 4;
    s8 hf[4];
#pragma unroll
    for (int ks = 0; ks < 4; ++ks) hf[ks] = *LDSP(s8, L1_H + (16 * rt + c) * 256 + (((4 * ks + q) ^ c) << 4));
    const u32 wb = L1_W2 + (8 * q + (c >> 2)) * 256 + (c & 3) * 8 + cg * 128;
#pragma unroll
    for (int cc = 0; cc < 4; ++cc) {
      f4 acc = {0.f, 0.f, 0.f, 0.f};
#pragma unroll
      for (int ks = 0; ks < 4; ++ks) acc = MF16(trfrag(wb + ks * 8192 + cc * 32, wb + ks * 8192 + cc * 32 + 1024), hf[ks], acc);
      u2 o; o[0] = pk2(acc[0] * NLOG2E, acc[1] * NLOG2E); o[1] = pk2(acc[2] * NLOG2E, acc[3] * NLOG2E);
      *LDSP(u2, L1_OUT + (16 * rt + c) * 272 + (cg * 4 + cc) * 32 + q * 8) = o;
    }
  }
  __syncthreads();
  STAMP(st, 2);
#pragma unroll
  for (int i = 0; i < 2; ++i) {
    const int x = t + 1024 * i;
    if (x < NB * CAP1) {
      const int b = x >> 3;
      u4 e = *LDSP(u4, L1_IMG + x * 16);
      e[0] = (e[0] & 0xe000ffffu) | (*LDSP(u32, L1_CNT + 4 * b) << 16);
      *(u4*)(ws + WS_S1 + ((size_t)(b * NG + g) * CAP1 + (x & 7)) * 16) = e;
    }
  }
  if (t < 640) *(u4*)(ws + WS_BT + (size_t)(g * NPB + 1) * 256 + t * 16) = *LDSP(u4, L1_OUT + (t >> 4) * 272 + (t & 15) * 16);
  STAMP(st, 3);
}

DI u32 place(const u4& e, bool valid) {
  u32 a2 = ~0u;
  if (valid) {
    const u32 blk = L2_S + ((e[3] >> 16) << 3);
    const u32 rank = __hip_atomic_fetch_add(LDSP(u32, blk + 2 * WCAP), 1u, __ATOMIC_RELAXED, __HIP_MEMORY_SCOPE_WORKGROUP);
    if (rank < WCAP) { a2 = blk + 2 * rank; *LDSP(u16, a2 + 22 * SP) = (u16)e[0]; }
  }
  return a2;
}
DI void fill(const u4& e, u32 a2) {
  if (a2 != ~0u) {
    const u32 a1 = a2 + (e[0] >> 29) * (4 * SP);
    *LDSP(u16, a1) = 0x3f80;
    *LDSP(u16, a1 + SP) = (u16)e[2];
    *LDSP(u16, a1 + 2 * SP) = (u16)(e[2] >> 16);
    *LDSP(u16, a1 + 3 * SP) = (u16)e[3];
    *LDSP(u16, a2 + 20 * SP) = (u16)e[1];
    *LDSP(u16, a2 + 21 * SP) = (u16)(e[1] >> 16);
  }
}
DI u32 silu2(float a, float b) {
  f2 t = {a, b}, e;
  e.x = __builtin_amdgcn_exp2f(a); e.y = __builtin_amdgcn_exp2f(b);
  e = e + 1.0f;
  e.x = __builtin_amdgcn_rcpf(e.x); e.y = __builtin_amdgcn_rcpf(e.y);
  e = e * t;
  return __builtin_bit_cast(u32, __builtin_convertvector(e, bf2));
}

__global__ __launch_bounds__(1024) void k2(const u16* __restrict__ h, const u16* __restrict__ pos, const u16* __restrict__ ea,
                                           const int* __restrict__ src, const int* __restrict__ dst, const u16* __restrict__ we,
                                           const u16* __restrict__ wx, char* __restrict__ ws, u16* __restrict__ out) {
  const int b = blockIdx.x, t = threadIdx.x, lane = t & 63, w = __builtin_amdgcn_readfirstlane(t >> 6);
  const int win = w >> 1, hf = w & 1;
  const int c32 = lane & 31, hh = lane >> 5, c16 = lane & 15, q4 = lane >> 4;
  STAMP(st, 0);
  const int sg = t >> 2, sj = t & 3;
  const __amdgpu_buffer_rsrc_t r1 = mkrsrc(ws + WS_S1 + (size_t)b * NG * CAP1 * 16, NG * CAP1 * 16);
  const __amdgpu_buffer_rsrc_t r2 = mkrsrc(ws + WS_S2 + (size_t)b * NG * CAP2 * 16, NG * CAP2 * 16);
  const __amdgpu_buffer_rsrc_t rh = mkrsrc(h + (size_t)b * NPB * 128, NPB * 256);
  const u4 e0 = ld16(r1, sg * 128 + sj * 16), e1 = ld16(r1, sg * 128 + sj * 16 + 64);
  const u4 hv = ld16(rh, t * 16);
  s8 wf[4];
#pragma unroll
  for (int ks = 0; ks < 4; ++ks) wf[ks] = *(const s8*)(ws + WS_WT + (w >> 3) * 32768 + ((w & 7) * 4 + ks) * 1024 + lane * 16);
  u32 wd[2], wa[2];
#pragma unroll
  for (int ct = 0; ct < 2; ++ct) { wd[ct] = we[256 * 128 + 64 * hf + 32 * ct + c32]; wa[ct] = we[257 * 128 + 64 * hf + 32 * ct + c32]; }
  {
    const u4 z = {0, 0, 0, 0};
#pragma unroll
    for (int i = 0; i < 6; ++i) { const int x = t + 1024 * i; if (x < L2_STG / 16) *LDSP(u4, x * 16) = z; }
  }
  if (t < 640) { const int n = t >> 4; *LDSP(u4, L2_STG + (n >> 2) * STG + 2048 + (n & 3) * 272 + (t & 15) * 16) = hv; }
  __syncthreads();
  STAMP(st, 1);
#pragma unroll
  for (int rt = 0; rt < 3; ++rt) {
    const int n = 16 * rt + c16;
    f4 acc = {0.f, 0.f, 0.f, 0.f};
#pragma unroll
    for (int ks = 0; ks < 4; ++ks) acc = MF16(wf[ks], *LDSP(s8, L2_STG + (n >> 2) * STG + 2048 + (n & 3) * 272 + ks * 64 + q4 * 16), acc);
    if (n < NPB) {
      if (w < 8) {
        const int nwin = (n * 205) >> 10, nw = n - 5 * nwin;
        *LDSP(f4, L2_STG + (nwin * 2 + (w >> 2)) * STG + nw * 256 + (w & 3) * 64 + q4 * 16) = acc * NLOG2E;
      } else {
        u2 o; o[0] = pk2(acc[0], acc[1]); o[1] = pk2(acc[2], acc[3]);
        *LDSP(u2, L2_PARK + ((w - 8) * NPB + n) * 32 + q4 * 8) = o;
      }
    }
  }
  const u32 cnt = (e0[0] >> 16) & 0x1fffu;
  const u32 o2 = sg * (CAP2 * 16) + sj * 16;
  const u4 e2 = ld16(r2, sj + 8 < cnt ? o2 : 0x7ffffff0u), e3 = ld16(r2, sj + 12 < cnt ? o2 + 64 : 0x7ffffff0u);
  const u32 a0 = place(e0, sj < cnt), a1 = place(e1, sj + 4 < cnt);
  if (cnt > CAP1 + CAP2) *LDSP(u32, L2_FLAG) = 1;
  __syncthreads();
  STAMP(st, 2);
  const u32 stg = L2_STG + w * STG, sblk = L2_S + win * SBLK;
  s8 wxf[2];
#pragma unroll
  for (int ct = 0; ct < 2; ++ct) {
    float v[8];
    v[0] = bflo(wd[ct]) * NLOG2E; v[1] = bflo(wa[ct]) * NLOG2E; v[7] = 0.f;
#pragma unroll
    for (int i = 0; i < 5; ++i) v[2 + i] = *LDSP(float, stg + i * 256 + (32 * ct + c32) * 4);
    u4 f;
#pragma unroll
    for (int j = 0; j < 4; ++j) {
      const u32 hi = pk2(v[2 * j], v[2 * j + 1]);
      const u32 lo = pk2(v[2 * j] - bflo(hi), v[2 * j + 1] - bfhi(hi));
      f[j] = hh ? lo : hi;
    }
    wxf[ct] = __builtin_bit_cast(s8, f);
  }
  const __amdgpu_buffer_rsrc_t rb = mkrsrc(ws + WS_BT, (NN + 1) * 256);
  const u32 lconst = hf * 128 + (((lane & 7) ^ (hh << 2)) << 4);
  const u32 idaddr = sblk + 22 * SP + (lane >> 3) * 8;
  auto gather = [&](u2 ids) {
    dma16(rb, stg, __builtin_amdgcn_perm(ids[0], lconst, 0x0c050400u));
    dma16(rb, stg + 1040, __builtin_amdgcn_perm(ids[0], lconst, 0x0c070600u));
    dma16(rb, stg + 2080, __builtin_amdgcn_perm(ids[1], lconst, 0x0c050400u));
    dma16(rb, stg + 3120, __builtin_amdgcn_perm(ids[1], lconst, 0x0c070600u));
  };
  u2 ids = *LDSP(u2, idaddr);
  const bool unset = (ids[0] & 0xffffu) == 0 || (ids[0] >> 16) == 0 || (ids[1] & 0xffffu) == 0 || (ids[1] >> 16) == 0;
  const bool redo = __builtin_amdgcn_ballot_w64(unset) != 0;
  asm volatile("s_waitcnt lgkmcnt(0)" ::: "memory");
  if (!redo) gather(ids);
  fill(e0, a0); fill(e1, a1);
  fill(e2, place(e2, sj + 8 < cnt));
  fill(e3, place(e3, sj + 12 < cnt));
#pragma unroll 1
  for (int i = 4; i < 8; ++i) {
    const bool v = sj + 4 * i < cnt;
    if (__builtin_amdgcn_ballot_w64(v) == 0) break;
    const u4 e = ld16(r2, v ? o2 + 64 * (i - 2) : 0x7ffffff0u);
    fill(e, place(e, v));
  }
  asm volatile("s_waitcnt lgkmcnt(0)" ::: "memory");
  __builtin_amdgcn_s_barrier();
  asm volatile("" ::: "memory");
  STAMP(st, 3);
  const int wc = __builtin_amdgcn_readfirstlane(*LDSP(u32, sblk + 2 * WCAP));
  const bool slow = wc > WCAP || __builtin_amdgcn_readfirstlane(*LDSP(u32, L2_FLAG)) != 0;
  float wxv[2];
  if (!slow) {
    if (redo) { ids = *LDSP(u2, idaddr); gather(ids); }
    f16v y0, y1;
#pragma unroll
    for (int i = 0; i < 16; ++i) { y0[i] = 0.f; y1[i] = 0.f; }
    s8 id0, id1;
#pragma unroll
    for (int j = 0; j < 8; ++j) { id0[j] = (c32 == 8 * hh + j) ? 0x3f80 : 0; id1[j] = (c32 == 16 + 8 * hh + j) ? 0x3f80 : 0; }
    const u32 ga = stg + (c32 & 3) * 1040 + (c32 >> 2) * 128 + hh * 16 + ((c32 >> 4) << 6);
    const u32 gb = stg + (c32 & 3) * 1040 + (c32 >> 2) * 128 + hh * 16 + (((c32 >> 4) ^ 1) << 6);
    const int xq = (lane >> 2) & 3;
    u32 xa = sblk + (xq < 2 ? 20 + xq : 4 * xq - 8) * SP + (q4 & 1) * 32 + (lane & 3) * 8;
    u32 xb = sblk + (xq < 3 ? 8 + 4 * xq : 16) * SP + (q4 & 1) * 32 + (lane & 3) * 8;
    u32 sa = sblk + (c32 < 23 ? c32 : 22) * SP + hh * 8;
    u32 ia = idaddr;
    f16v zero;
#pragma unroll
    for (int i = 0; i < 16; ++i) zero[i] = 0.f;
    for (int rem = wc; rem > 0; rem -= 32) {
      asm volatile("s_waitcnt vmcnt(0)" ::: "memory");
      const s8 g0 = *LDSP(s8, ga), g1 = *LDSP(s8, ga + 32), g2 = *LDSP(s8, gb), g3 = *LDSP(s8, gb + 32);
      const s8 xf = trfrag(xa, xb);
      ia += 64;
      const u2 nids = *LDSP(u2, ia);
      const s4 s00 = *LDSP(s4, sa), s01 = *LDSP(s4, sa + 16), s10 = *LDSP(s4, sa + 32), s11 = *LDSP(s4, sa + 48);
      f16v d0 = MF32(xf, wxf[0], zero);
      d0 = MF32(g0, id0, d0);
      d0 = MF32(g1, id1, d0);
      f16v d1 = MF32(xf, wxf[1], zero);
      d1 = MF32(g2, id0, d1);
      d1 = MF32(g3, id1, d1);
      asm volatile("s_waitcnt lgkmcnt(0)" ::: "memory");
      if (rem > 32) gather(nids);
      xa += 64; xb += 64; sa += 64;
      u4 p0 = {0, 0, 0, 0}, p1 = {0, 0, 0, 0};
      p0[0] = silu2(d0[0], d0[1]); p0[1] = silu2(d0[2], d0[3]);
      p1[0] = silu2(d1[0], d1[1]); p1[1] = silu2(d1[2], d1[3]);
      if (rem > 8) {
        p0[2] = silu2(d0[4], d0[5]); p0[3] = silu2(d0[6], d0[7]);
        p1[2] = silu2(d1[4], d1[5]); p1[3] = silu2(d1[6], d1[7]);
      }
      const s8 sf0 = __builtin_shufflevector(s00, s01, 0, 1, 2, 3, 4, 5, 6, 7);
      y0 = MF32(sf0, __builtin_bit_cast(s8, p0), y0);
      y1 = MF32(sf0, __builtin_bit_cast(s8, p1), y1);
      if (rem > 16) {
        u4 r0 = {0, 0, 0, 0}, r1 = {0, 0, 0, 0};
        r0[0] = silu2(d0[8], d0[9]); r0[1] = silu2(d0[10], d0[11]);
        r1[0] = silu2(d1[8], d1[9]); r1[1] = silu2(d1[10], d1[11]);
        if (rem > 24) {
          r0[2] = silu2(d0[12], d0[13]); r0[3] = silu2(d0[14], d0[15]);
          r1[2] = silu2(d1[12], d1[13]); r1[3] = silu2(d1[14], d1[15]);
        }
        const s8 sf1 = __builtin_shufflevector(s10, s11, 0, 1, 2, 3, 4, 5, 6, 7);
        y0 = MF32(sf1, __builtin_bit_cast(s8, r0), y0);
        y1 = MF32(sf1, __builtin_bit_cast(s8, r1), y1);
      }
    }
    STAMP(st, 4);
    asm volatile("s_waitcnt vmcnt(0)" ::: "memory");
    wxv[0] = bflo(wx[64 * hf + c32]) * NLN2; wxv[1] = bflo(wx[64 * hf + 32 + c32]) * NLN2;
#pragma unroll
    for (int g4 = 0; g4 < 3; ++g4) {
      const u32 m = pk2(y0[4 * g4] * NLN2, y1[4 * g4] * NLN2);
      const u32 a = stg + (2 * g4 + hh) * MP + c32 * 2;
      *LDSP(u16, a) = (u16)m; *LDSP(u16, a + 64) = (u16)(m >> 16);
#pragma unroll
      for (int k = 1; k < 4; ++k) *LDSP(float, stg + PART + (g4 * 3 + k - 1) * 256 + lane * 4) = y0[4 * g4 + k] * wxv[0] + y1[4 * g4 + k] * wxv[1];
    }
  } else {
    asm volatile("s_waitcnt vmcnt(0)" ::: "memory");
    const int ch = 64 * hf + lane, n0 = b * NPB + 5 * win;
    float av[5], acc[5][4];
#pragma unroll
    for (int i = 0; i < 5; ++i) {
      float a = 0.f;
#pragma unroll 1
      for (int k = 0; k < 128; ++k) a += bflo(h[(n0 + i) * 128 + k]) * bflo(we[k * 128 + ch]);
      av[i] = a * NLOG2E;
#pragma unroll
      for (int k = 0; k < 4; ++k) acc[i][k] = 0.f;
    }
    const float wdv = bflo(we[256 * 128 + ch]) * NLOG2E, wav = bflo(we[257 * 128 + ch]) * NLOG2E;
#pragma unroll 1
    for (int eb = 0; eb < EE; eb += 64) {
      const int dl = dst[eb + lane] - n0;
      unsigned long long mk = __builtin_amdgcn_ballot_w64((unsigned)dl < 5u);
      while (mk) {
        const int l = __builtin_ctzll(mk); mk &= mk - 1;
        const int e = eb + l, nw = __builtin_amdgcn_readlane(dl, l), s = src[e], d = n0 + nw;
        u32 w2, dz, d2;
        geom(pos[3 * d] | ((u32)pos[3 * d + 1] << 16), pos[3 * d + 2], pos[3 * s] | ((u32)pos[3 * s + 1] << 16), pos[3 * s + 2], w2, dz, d2);
        const float tb = bflo(((const u16*)(ws + WS_BT))[(s + 1) * 128 + ch]) + bflo(d2) * wdv + bflo(ea[e]) * wav;
#pragma unroll
        for (int i = 0; i < 5; ++i)
          if (nw == i) {
            const float m = bflo(silu2(av[i] + tb, 0.f));
            acc[i][0] += m; acc[i][1] += m * bflo(w2); acc[i][2] += m * bfhi(w2); acc[i][3] += m * bflo(dz);
          }
      }
    }
    const float wxs = bflo(wx[ch]) * NLN2;
#pragma unroll
    for (int i = 0; i < 5; ++i) {
      *LDSP(u16, stg + i * MP + lane * 2) = (u16)pk2(acc[i][0] * NLN2, 0.f);
#pragma unroll
      for (int k = 1; k < 4; ++k) {
        float v = acc[i][k] * wxs;
        v += __shfl_xor(v, 32);
        if (hh == (i & 1)) *LDSP(float, stg + PART + ((i >> 1) * 3 + k - 1) * 256 + lane * 4) = v;
      }
    }
  }
  const int t2 = w * 64 + __builtin_amdgcn_mbcnt_hi(~0u, __builtin_amdgcn_mbcnt_lo(~0u, 0u));
  const u4 hv2 = ld16(rh, t2 * 16);
  const __amdgpu_buffer_rsrc_t rw = mkrsrc(ws + WS_WT + 2 * 32768, w >= 8 ? 32768 : 0);
  u4 wf2[4];
#pragma unroll
  for (int ks = 0; ks < 4; ++ks) wf2[ks] = ld16(rw, ((w & 7) * 4 + ks) * 1024 + lane * 16);
  const u32 pv = pos[(b * NPB + 5 * (w & 7)) * 3 + min(lane >> 2, 14)];
  asm volatile("s_waitcnt lgkmcnt(0)" ::: "memory");
  __builtin_amdgcn_s_barrier();
  asm volatile("" ::: "memory");
  STAMP(st, 5);
  if (w >= 8) {
#pragma unroll
    for (int rt = 0; rt < 3; ++rt) {
      const int n = min(16 * rt + c16, NPB - 1), nwin = (n * 205) >> 10, nw = n - 5 * nwin;
      const u32 pa = L2_PARK + ((w - 8) * NPB + n) * 32 + q4 * 8;
      const u2 pk = *LDSP(u2, pa);
      f4 acc = {bflo(pk[0]), bfhi(pk[0]), bflo(pk[1]), bfhi(pk[1])};
      const u32 ma = L2_STG + nwin * 2 * STG + nw * MP + q4 * 16;
#pragma unroll
      for (int ks = 0; ks < 4; ++ks) acc = MF16(__builtin_bit_cast(s8, wf2[ks]), *LDSP(s8, ma + (ks >> 1) * STG + (ks & 1) * 64), acc);
      f4 o;
#pragma unroll
      for (int j = 0; j < 4; ++j) o[j] = acc[j] * __builtin_amdgcn_rcpf(1.0f + __builtin_amdgcn_exp2f(acc[j] * NLOG2E));
      u2 ov; ov[0] = pk2(o[0], o[1]); ov[1] = pk2(o[2], o[3]);
      if (16 * rt + c16 < NPB) *LDSP(u2, pa) = ov;
    }
  } else {
    const int o = min(lane >> 2, 14), nw = o / 3, k = o - 3 * nw, u = lane & 3;
    const u32 a = L2_STG + (2 * w + (u >> 1)) * STG + PART + ((nw >> 1) * 3 + k) * 256 + (nw & 1) * 128 + (u & 1) * 64;
    const f4 v = *LDSP(f4, a) + *LDSP(f4, a + 16) + *LDSP(f4, a + 32) + *LDSP(f4, a + 48);
    float s = (v[0] + v[1]) + (v[2] + v[3]);
    s += __shfl_xor(s, 1); s += __shfl_xor(s, 2);
    if (lane < 60 && u == 0) out[NN * 128 + (b * NPB + 5 * w) * 3 + o] = (u16)pk2(bflo(pv) + s, 0.f);
  }
  __syncthreads();
  if (t < 640) {
    const u4 sv = *LDSP(u4, L2_PARK + (((t & 15) >> 1) * NPB + (t >> 4)) * 32 + (t & 1) * 16);
    u4 o;
#pragma unroll
    for (int j = 0; j < 4; ++j) o[j] = pk2(bflo(hv2[j]) + bflo(sv[j]), bfhi(hv2[j]) + bfhi(sv[j]));
    *(u4*)(out + (size_t)b * NPB * 128 + t * 8) = o;
  }
  STAMP(st, 6);
}

extern "C" void kernel_launch(void* const* d_in, const int* in_sizes, int n_in, void* d_out, int out_size, void* d_ws, size_t ws_size,
                              hipStream_t stream) {
  const u16* h = (const u16*)d_in[0]; const u16* pos = (const u16*)d_in[1]; const u16* ea = (const u16*)d_in[2];
  const int* src = (const int*)d_in[3]; const int* dst = (const int*)d_in[4];
  const u16* we = (const u16*)d_in[5]; const u16* wx = (const u16*)d_in[6]; const u16* wh = (const u16*)d_in[7];
  (void)hipFuncSetAttribute((const void*)k1, hipFuncAttributeMaxDynamicSharedMemorySize, L1_END);
  (void)hipFuncSetAttribute((const void*)k2, hipFuncAttributeMaxDynamicSharedMemorySize, L2_END);
  k1<<<NG + 3, 1024, L1_END, stream>>>(h, pos, ea, src, dst, we, wh, (char*)d_ws);
  k2<<<NB, 1024, L2_END, stream>>>(h, pos, ea, src, dst, we, wx, (char*)d_ws, (u16*)d_out);
}
```

```cpp
#include <hip/hip_runtime.h>
#include <stdint.h>

typedef unsigned short u16;
typedef unsigned int u32;
typedef __attribute__((ext_vector_type(8))) short s8;
typedef __attribute__((ext_vector_type(4))) short s4;
typedef __attribute__((ext_vector_type(2))) float f2;
typedef __attribute__((ext_vector_type(4))) float f4;
typedef __attribute__((ext_vector_type(16))) float f16v;
typedef __attribute__((ext_vector_type(2))) __bf16 bf2;
typedef __attribute__((ext_vector_type(2))) u32 u2;
typedef __attribute__((ext_vector_type(4))) u32 u4;
#define LDSP(T, a) ((__attribute__((address_space(3))) T*)(size_t)(a))
#define DI __device__ __forceinline__

constexpr int NN = 10000, EE = 320000;
constexpr int NB = 250, NPB = 40, NG = 250, CH = 1280, CAP1 = 8, CAP2 = 24;
constexpr float NLOG2E = -1.4426950408889634f, NLN2 = -0.6931471805599453f;
constexpr size_t WS_BT = 0;
constexpr size_t WS_WT = 2560256 + 3840;
constexpr size_t WS_S1 = WS_WT + 3 * 32768;
constexpr size_t WS_S2 = WS_S1 + (size_t)NB * NG * CAP1 * 16;
constexpr size_t WS_END = WS_S2 + (size_t)NB * NG * CAP2 * 16;
constexpr int L1_POS = 0, L1_W2 = 60416, L1_H = L1_W2 + 32768, L1_IMG = L1_H + 12288, L1_CNT = L1_IMG + 32000,
              L1_OUT = L1_CNT + 1024, L1_END = L1_OUT + 48 * 272;
constexpr int WCAP = 224, SP = 456, SBLK = 23 * SP;
constexpr int L2_S = 0, L2_FLAG = 8 * SBLK, L2_STG = L2_FLAG + 16, STG = 4160, L2_PARK = L2_STG + 16 * STG,
              L2_END = L2_PARK + 10240;
constexpr int MP = 144, PART = 1024;
static_assert(L2_STG % 16 == 0 && L2_END <= 163840 && L1_END <= 163840, "lds");

#define STAMP_DECL
#define STAMP(buf, i)
#define STAMP_END(buf, n)

DI float bflo(u32 w) { return __uint_as_float(w << 16); }
DI float bfhi(u32 w) { return __uint_as_float(w & 0xffff0000u); }
DI u32 pk2(float a, float b) { f2 v; v.x = a; v.y = b; return __builtin_bit_cast(u32, __builtin_convertvector(v, bf2)); }
DI __amdgpu_buffer_rsrc_t mkrsrc(const void* p, int bytes) { return __builtin_amdgcn_make_buffer_rsrc((void*)p, 0, bytes, 0x00020000); }
DI void dma16(__amdgpu_buffer_rsrc_t r, u32 lds, u32 voff) { __builtin_amdgcn_raw_ptr_buffer_load_lds(r, LDSP(void, lds), 16, voff, 0, 0, 0); }
DI u4 ld16(__amdgpu_buffer_rsrc_t r, u32 voff) { return __builtin_amdgcn_raw_buffer_load_b128(r, voff, 0, 0); }
DI s8 trfrag(u32 a0, u32 a1) {
  s4 lo = __builtin_amdgcn_ds_read_tr16_b64_v4i16(LDSP(s4, a0));
  s4 hi = __builtin_amdgcn_ds_read_tr16_b64_v4i16(LDSP(s4, a1));
  return __builtin_shufflevector(lo, hi, 0, 1, 2, 3, 4, 5, 6, 7);
}
#define MF16(a, b, c) __builtin_amdgcn_mfma_f32_16x16x32_bf16((a), (b), (c), 0, 0, 0)
#define MF32(a, b, c) __builtin_amdgcn_mfma_f32_32x32x16_bf16((a), (b), (c), 0, 0, 0)

DI void geom(u32 dxy, u32 dz_, u32 sxy, u32 sz_, u32& w2, u32& dz, u32& d2) {
  w2 = pk2(bflo(dxy) - bflo(sxy), bfhi(dxy) - bfhi(sxy));
  dz = pk2(bflo(dz_) - bflo(sz_), 0.f);
  const float x = bflo(w2), y = bfhi(w2), z = bflo(dz);
  const u32 q = pk2(x * x, y * y), qz = pk2(z * z, 0.f);
  d2 = pk2(bflo(q) + bfhi(q) + bflo(qz), 0.f);
}

__global__ __launch_bounds__(1024) void k1(const u16* __restrict__ h, const u16* __restrict__ pos, const u16* __restrict__ ea,
                                           const int* __restrict__ src, const int* __restrict__ dst, const u16* __restrict__ we,
                                           const u16* __restrict__ wh, char* __restrict__ ws) {
  const int g = blockIdx.x, t = threadIdx.x, lane = t & 63, w = __builtin_amdgcn_readfirstlane(t >> 6);
  STAMP_DECL
  STAMP(st, 0);
  if (g >= NG) {
    const int mat = g - NG;
    const u16* W = mat == 0 ? we : (mat == 1 ? wh : wh + 128 * 128);
#pragma unroll
    for (int i = 0; i < 2; ++i) {
      const int f = t + 1024 * i, ct = f >> 8, ks = (f >> 6) & 3;
      const u16* p = W + (32 * ks + 8 * (lane >> 4)) * 128 + 16 * ct + (lane & 15);
      u4 v;
#pragma unroll
      for (int j = 0; j < 4; ++j) v[j] = (u32)p[(2 * j) * 128] | ((u32)p[(2 * j + 1) * 128] << 16);
      *(u4*)(ws + WS_WT + mat * 32768 + f * 16) = v;
    }
    if (mat == 0 && t < 16) { u4 z = {0, 0, 0, 0}; *(u4*)(ws + WS_BT + t * 16) = z; }
    return;
  }
  u2 dv = {0, 0}, sv = {0, 0}; u32 ev = 0;
  if (w < 10) {
    const int e0 = g * CH + 2 * t;
    dv = *(const u2*)(dst + e0); sv = *(const u2*)(src + e0); ev = *(const u32*)(ea + e0);
  }
  {
    const __amdgpu_buffer_rsrc_t rp = mkrsrc(pos, NN * 6), rw = mkrsrc(we + 128 * 128, 32768), rh = mkrsrc(h + (size_t)g * NPB * 128, NPB * 256);
#pragma unroll
    for (int i = 0; i < 4; ++i) { const int p = w + 16 * i; if (p < 59) dma16(rp, L1_POS + p * 1024, p * 1024 + lane * 16); }
#pragma unroll
    for (int i = 0; i < 2; ++i) { const int p = w + 16 * i; dma16(rw, L1_W2 + p * 1024, p * 1024 + lane * 16); }
    if (w >= 4) { const int p = w - 4, row = 4 * p + (lane >> 4); dma16(rh, L1_H + p * 1024, row * 256 + (((lane & 15) ^ (row & 15)) << 4)); }
  }
  if (t < 256) *LDSP(u32, L1_CNT + 4 * t) = 0;
  asm volatile("s_waitcnt vmcnt(0)" ::: "memory");
  __syncthreads();
  STAMP(st, 1);
  if (w < 10) {
#pragma unroll
    for (int i = 0; i < 2; ++i) {
      const u32 d = dv[i], s = sv[i];
      const u32 ad = (d * 6) & ~3u, as = (s * 6) & ~3u;
      const u32 d0 = *LDSP(u32, L1_POS + ad), d1 = *LDSP(u32, L1_POS + ad + 4);
      const u32 s0 = *LDSP(u32, L1_POS + as), s1 = *LDSP(u32, L1_POS + as + 4);
      const u32 wg = (d * 52429u) >> 18, b = wg >> 3, nw = d - 5 * wg;
      const u32 rank = __hip_atomic_fetch_add(LDSP(u32, L1_CNT + 4 * b), 1u, __ATOMIC_RELAXED, __HIP_MEMORY_SCOPE_WORKGROUP);
      const u32 shd = (d & 1) << 4, shs = (s & 1) << 4;
      u32 w2, dz, d2;
      geom(__builtin_amdgcn_alignbit(d1, d0, shd), d1 >> shd, __builtin_amdgcn_alignbit(s1, s0, shs), s1 >> shs, w2, dz, d2);
      u4 e;
      e[0] = (s + 1) | (nw << 29);
      e[1] = (d2 & 0xffffu) | (i == 0 ? (ev << 16) : (ev & 0xffff0000u));
      e[2] = w2;
      e[3] = (dz & 0xffffu) | (((wg & 7) * (SBLK / 8)) << 16);
      if (rank < CAP1) *LDSP(u4, L1_IMG + (b * CAP1 + rank) * 16) = e;
      else if (rank < CAP1 + CAP2) *(u4*)(ws + WS_S2 + ((size_t)(b * NG + g) * CAP2 + (rank - CAP1)) * 16) = e;
    }
  } else {
    const int i = w - 10, rt = i % 3, cg = i / 3, c = lane & 15, q = lane >> 4;
    s8 hf[4];
#pragma unroll
    for (int ks = 0; ks < 4; ++ks) hf[ks] = *LDSP(s8, L1_H + (16 * rt + c) * 256 + (((4 * ks + q) ^ c) << 4));
    const u32 wb = L1_W2 + (8 * q + (c >> 2)) * 256 + (c & 3) * 8 + cg * 128;
#pragma unroll
    for (int cc = 0; cc < 4; ++cc) {
      f4 acc = {0.f, 0.f, 0.f, 0.f};
#pragma unroll
      for (int ks = 0; ks < 4; ++ks) acc = MF16(trfrag(wb + ks * 8192 + cc * 32, wb + ks * 8192 + cc * 32 + 1024), hf[ks], acc);
      u2 o; o[0] = pk2(acc[0] * NLOG2E, acc[1] * NLOG2E); o[1] = pk2(acc[2] * NLOG2E, acc[3] * NLOG2E);
      *LDSP(u2, L1_OUT + (16 * rt + c) * 272 + (cg * 4 + cc) * 32 + q * 8) = o;
    }
  }
  STAMP(st, 2);
  __syncthreads();
  STAMP(st, 3);
#pragma unroll
  for (int i = 0; i < 2; ++i) {
    const int x = t + 1024 * i;
    if (x < NB * CAP1) {
      const int b = x >> 3;
      u4 e = *LDSP(u4, L1_IMG + x * 16);
      e[0] = (e[0] & 0xe000ffffu) | (*LDSP(u32, L1_CNT + 4 * b) << 16);
      __builtin_amdgcn_raw_buffer_store_b128(e, mkrsrc(ws + WS_S1, NB * NG * CAP1 * 16), ((b * NG + g) * CAP1 + (x & 7)) * 16, 0, 2);
    }
  }
  if (t < 640) __builtin_amdgcn_raw_buffer_store_b128(*LDSP(u4, L1_OUT + (t >> 4) * 272 + (t & 15) * 16), mkrsrc(ws + WS_BT, (NN + 1) * 256), (g * NPB + 1) * 256 + t * 16, 0, 2);
  STAMP(st, 4);
  STAMP(st, 5);
  STAMP_END(st, 6);
}

DI u32 place(const u4& e, bool valid) {
  u32 a2 = ~0u;
  if (valid) {
    const u32 blk = L2_S + ((e[3] >> 16) << 3);
    const u32 rank = __hip_atomic_fetch_add(LDSP(u32, blk + 2 * WCAP), 1u, __ATOMIC_RELAXED, __HIP_MEMORY_SCOPE_WORKGROUP);
    if (rank < WCAP) { a2 = blk + 2 * rank; *LDSP(u16, a2 + 22 * SP) = (u16)e[0]; }
  }
  return a2;
}
DI void fill(const u4& e, u32 a2) {
  if (a2 != ~0u) {
    const u32 a1 = a2 + (e[0] >> 29) * (4 * SP);
    *LDSP(u16, a1) = 0x3f80;
    *LDSP(u16, a1 + SP) = (u16)e[2];
    *LDSP(u16, a1 + 2 * SP) = (u16)(e[2] >> 16);
    *LDSP(u16, a1 + 3 * SP) = (u16)e[3];
    *LDSP(u16, a2 + 20 * SP) = (u16)e[1];
    *LDSP(u16, a2 + 21 * SP) = (u16)(e[1] >> 16);
  }
}
DI u32 silu2(float a, float b) {
  f2 t = {a, b}, e;
  e.x = __builtin_amdgcn_exp2f(a); e.y = __builtin_amdgcn_exp2f(b);
  e = e + 1.0f;
  e.x = __builtin_amdgcn_rcpf(e.x); e.y = __builtin_amdgcn_rcpf(e.y);
  e = e * t;
  return __builtin_bit_cast(u32, __builtin_convertvector(e, bf2));
}

__global__ __launch_bounds__(1024) void k2(const u16* __restrict__ h, const u16* __restrict__ pos, const u16* __restrict__ ea,
                                           const int* __restrict__ src, const int* __restrict__ dst, const u16* __restrict__ we,
                                           const u16* __restrict__ wx, char* __restrict__ ws, u16* __restrict__ out) {
  const int b = blockIdx.x, t = threadIdx.x, lane = t & 63, w = __builtin_amdgcn_readfirstlane(t >> 6);
  const int win = w >> 1, hf = w & 1;
  const int c32 = lane & 31, hh = lane >> 5, c16 = lane & 15, q4 = lane >> 4;
  STAMP_DECL
  STAMP(st, 0);
  const int sg = t >> 2, sj = t & 3;
  const __amdgpu_buffer_rsrc_t r1 = mkrsrc(ws + WS_S1 + (size_t)b * NG * CAP1 * 16, NG * CAP1 * 16);
  const __amdgpu_buffer_rsrc_t r2 = mkrsrc(ws + WS_S2 + (size_t)b * NG * CAP2 * 16, NG * CAP2 * 16);
  const __amdgpu_buffer_rsrc_t rh = mkrsrc(h + (size_t)b * NPB * 128, NPB * 256);
  const u4 e0 = ld16(r1, sg * 128 + sj * 16), e1 = ld16(r1, sg * 128 + sj * 16 + 64);
  const u4 hv = ld16(rh, t * 16);
  s8 wf[4];
#pragma unroll
  for (int ks = 0; ks < 4; ++ks) wf[ks] = *(const s8*)(ws + WS_WT + (w >> 3) * 32768 + ((w & 7) * 4 + ks) * 1024 + lane * 16);
  u32 wd[2], wa[2];
#pragma unroll
  for (int ct = 0; ct < 2; ++ct) { wd[ct] = we[256 * 128 + 64 * hf + 32 * ct + c32]; wa[ct] = we[257 * 128 + 64 * hf + 32 * ct + c32]; }
  {
    const u4 z = {0, 0, 0, 0};
#pragma unroll
    for (int i = 0; i < 6; ++i) { const int x = t + 1024 * i; if (x < L2_STG / 16) *LDSP(u4, x * 16) = z; }
  }
  STAMP(st, 1);
  if (t < 640) { const int n = t >> 4; *LDSP(u4, L2_STG + (n >> 2) * STG + 2048 + (n & 3) * 272 + (t & 15) * 16) = hv; }
  __syncthreads();
  STAMP(st, 2);
#pragma unroll
  for (int rt = 0; rt < 3; ++rt) {
    const int n = 16 * rt + c16;
    f4 acc = {0.f, 0.f, 0.f, 0.f};
#pragma unroll
    for (int ks = 0; ks < 4; ++ks) acc = MF16(wf[ks], *LDSP(s8, L2_STG + (n >> 2) * STG + 2048 + (n & 3) * 272 + ks * 64 + q4 * 16), acc);
    if (n < NPB) {
      if (w < 8) {
        const int nwin = (n * 205) >> 10, nw = n - 5 * nwin;
        *LDSP(f4, L2_STG + (nwin * 2 + (w >> 2)) * STG + nw * 256 + (w & 3) * 64 + q4 * 16) = acc * NLOG2E;
      } else {
        u2 o; o[0] = pk2(acc[0], acc[1]); o[1] = pk2(acc[2], acc[3]);
        *LDSP(u2, L2_PARK + ((w - 8) * NPB + n) * 32 + q4 * 8) = o;
      }
    }
  }
  STAMP(st, 3);
  const u32 cnt = (e0[0] >> 16) & 0x1fffu;
  const u32 o2 = sg * (CAP2 * 16) + sj * 16;
  const u4 e2 = ld16(r2, sj + 8 < cnt ? o2 : 0x7ffffff0u), e3 = ld16(r2, sj + 12 < cnt ? o2 + 64 : 0x7ffffff0u);
  const u32 a0 = place(e0, sj < cnt), a1 = place(e1, sj + 4 < cnt);
  if (cnt > CAP1 + CAP2) *LDSP(u32, L2_FLAG) = 1;
  STAMP(st, 4);
  __syncthreads();
  STAMP(st, 5);
  const u32 stg = L2_STG + w * STG, sblk = L2_S + win * SBLK;
  s8 wxf[2];
#pragma unroll
  for (int ct = 0; ct < 2; ++ct) {
    float v[8];
    v[0] = bflo(wd[ct]) * NLOG2E; v[1] = bflo(wa[ct]) * NLOG2E; v[7] = 0.f;
#pragma unroll
    for (int i = 0; i < 5; ++i) v[2 + i] = *LDSP(float, stg + i * 256 + (32 * ct + c32) * 4);
    u4 f;
#pragma unroll
    for (int j = 0; j < 4; ++j) {
      const u32 hi = pk2(v[2 * j], v[2 * j + 1]);
      const u32 lo = pk2(v[2 * j] - bflo(hi), v[2 * j + 1] - bfhi(hi));
      f[j] = hh ? lo : hi;
    }
    wxf[ct] = __builtin_bit_cast(s8, f);
  }
  const __amdgpu_buffer_rsrc_t rb = mkrsrc(ws + WS_BT, (NN + 1) * 256);
  const u32 lconst = hf * 128 + (((lane & 7) ^ (hh << 2)) << 4);
  const u32 idaddr = sblk + 22 * SP + (lane >> 3) * 8;
  auto gather = [&](u2 ids) {
    dma16(rb, stg, __builtin_amdgcn_perm(ids[0], lconst, 0x0c050400u));
    dma16(rb, stg + 1040, __builtin_amdgcn_perm(ids[0], lconst, 0x0c070600u));
    dma16(rb, stg + 2080, __builtin_amdgcn_perm(ids[1], lconst, 0x0c050400u));
    dma16(rb, stg + 3120, __builtin_amdgcn_perm(ids[1], lconst, 0x0c070600u));
  };
  u2 ids = *LDSP(u2, idaddr);
  const bool unset = (ids[0] & 0xffffu) == 0 || (ids[0] >> 16) == 0 || (ids[1] & 0xffffu) == 0 || (ids[1] >> 16) == 0;
  const bool redo = __builtin_amdgcn_ballot_w64(unset) != 0;
  asm volatile("s_waitcnt lgkmcnt(0)" ::: "memory");
  if (!redo) gather(ids);
  STAMP(st, 6);
  fill(e0, a0); fill(e1, a1);
  STAMP(st, 7);
  fill(e2, place(e2, sj + 8 < cnt));
  fill(e3, place(e3, sj + 12 < cnt));
#pragma unroll 1
  for (int i = 4; i < 8; ++i) {
    const bool v = sj + 4 * i < cnt;
    if (__builtin_amdgcn_ballot_w64(v) == 0) break;
    const u4 e = ld16(r2, v ? o2 + 64 * (i - 2) : 0x7ffffff0u);
    fill(e, place(e, v));
  }
  asm volatile("s_waitcnt lgkmcnt(0)" ::: "memory");
  __builtin_amdgcn_s_barrier();
  asm volatile("" ::: "memory");
  STAMP(st, 8);
  const int wc = __builtin_amdgcn_readfirstlane(*LDSP(u32, sblk + 2 * WCAP));
  const bool slow = wc > WCAP || __builtin_amdgcn_readfirstlane(*LDSP(u32, L2_FLAG)) != 0;
  float wxv[2];
  if (!slow) {
    if (redo) { ids = *LDSP(u2, idaddr); gather(ids); }
    f16v y0, y1;
#pragma unroll
    for (int i = 0; i < 16; ++i) { y0[i] = 0.f; y1[i] = 0.f; }
    s8 id0, id1;
#pragma unroll
    for (int j = 0; j < 8; ++j) { id0[j] = (c32 == 8 * hh + j) ? 0x3f80 : 0; id1[j] = (c32 == 16 + 8 * hh + j) ? 0x3f80 : 0; }
    const u32 ga = stg + (c32 & 3) * 1040 + (c32 >> 2) * 128 + hh * 16 + ((c32 >> 4) << 6);
    const u32 gb = stg + (c32 & 3) * 1040 + (c32 >> 2) * 128 + hh * 16 + (((c32 >> 4) ^ 1) << 6);
    const int xq = (lane >> 2) & 3;
    u32 xa = sblk + (xq < 2 ? 20 + xq : 4 * xq - 8) * SP + (q4 & 1) * 32 + (lane & 3) * 8;
    u32 xb = sblk + (xq < 3 ? 8 + 4 * xq : 16) * SP + (q4 & 1) * 32 + (lane & 3) * 8;
    u32 sa = sblk + (c32 < 23 ? c32 : 22) * SP + hh * 8;
    u32 ia = idaddr;
    f16v zero;
#pragma unroll
    for (int i = 0; i < 16; ++i) zero[i] = 0.f;
    STAMP(st, 9);
    for (int rem = wc; rem > 0; rem -= 32) {
      asm volatile("s_waitcnt vmcnt(0)" ::: "memory");
      const s8 g0 = *LDSP(s8, ga), g1 = *LDSP(s8, ga + 32), g2 = *LDSP(s8, gb), g3 = *LDSP(s8, gb + 32);
      const s8 xf = trfrag(xa, xb);
      ia += 64;
      const u2 nids = *LDSP(u2, ia);
      const s4 s00 = *LDSP(s4, sa), s01 = *LDSP(s4, sa + 16), s10 = *LDSP(s4, sa + 32), s11 = *LDSP(s4, sa + 48);
      f16v d0 = MF32(xf, wxf[0], zero);
      d0 = MF32(g0, id0, d0);
      d0 = MF32(g1, id1, d0);
      f16v d1 = MF32(xf, wxf[1], zero);
      d1 = MF32(g2, id0, d1);
      d1 = MF32(g3, id1, d1);
      asm volatile("s_waitcnt lgkmcnt(0)" ::: "memory");
      if (rem > 32) gather(nids);
      xa += 64; xb += 64; sa += 64;
      u4 p0 = {0, 0, 0, 0}, p1 = {0, 0, 0, 0};
      p0[0] = silu2(d0[0], d0[1]); p0[1] = silu2(d0[2], d0[3]);
      p1[0] = silu2(d1[0], d1[1]); p1[1] = silu2(d1[2], d1[3]);
      if (rem > 8) {
        p0[2] = silu2(d0[4], d0[5]); p0[3] = silu2(d0[6], d0[7]);
        p1[2] = silu2(d1[4], d1[5]); p1[3] = silu2(d1[6], d1[7]);
      }
      const s8 sf0 = __builtin_shufflevector(s00, s01, 0, 1, 2, 3, 4, 5, 6, 7);
      y0 = MF32(sf0, __builtin_bit_cast(s8, p0), y0);
      y1 = MF32(sf0, __builtin_bit_cast(s8, p1), y1);
      if (rem > 16) {
        u4 r0 = {0, 0, 0, 0}, r1 = {0, 0, 0, 0};
        r0[0] = silu2(d0[8], d0[9]); r0[1] = silu2(d0[10], d0[11]);
        r1[0] = silu2(d1[8], d1[9]); r1[1] = silu2(d1[10], d1[11]);
        if (rem > 24) {
          r0[2] = silu2(d0[12], d0[13]); r0[3] = silu2(d0[14], d0[15]);
          r1[2] = silu2(d1[12], d1[13]); r1[3] = silu2(d1[14], d1[15]);
        }
        const s8 sf1 = __builtin_shufflevector(s10, s11, 0, 1, 2, 3, 4, 5, 6, 7);
        y0 = MF32(sf1, __builtin_bit_cast(s8, r0), y0);
        y1 = MF32(sf1, __builtin_bit_cast(s8, r1), y1);
      }
    }
    STAMP(st, 10);
    asm volatile("s_waitcnt vmcnt(0)" ::: "memory");
    wxv[0] = bflo(wx[64 * hf + c32]) * NLN2; wxv[1] = bflo(wx[64 * hf + 32 + c32]) * NLN2;
#pragma unroll
    for (int g4 = 0; g4 < 3; ++g4) {
      const u32 m = pk2(y0[4 * g4] * NLN2, y1[4 * g4] * NLN2);
      const u32 a = stg + (2 * g4 + hh) * MP + c32 * 2;
      *LDSP(u16, a) = (u16)m; *LDSP(u16, a + 64) = (u16)(m >> 16);
#pragma unroll
      for (int k = 1; k < 4; ++k) *LDSP(float, stg + PART + (g4 * 3 + k - 1) * 256 + lane * 4) = y0[4 * g4 + k] * wxv[0] + y1[4 * g4 + k] * wxv[1];
    }
  } else {
    asm volatile("s_waitcnt vmcnt(0)" ::: "memory");
    const int ch = 64 * hf + lane, n0 = b * NPB + 5 * win;
    float av[5], acc[5][4];
#pragma unroll
    for (int i = 0; i < 5; ++i) {
      float a = 0.f;
#pragma unroll 1
      for (int k = 0; k < 128; ++k) a += bflo(h[(n0 + i) * 128 + k]) * bflo(we[k * 128 + ch]);
      av[i] = a * NLOG2E;
#pragma unroll
      for (int k = 0; k < 4; ++k) acc[i][k] = 0.f;
    }
    const float wdv = bflo(we[256 * 128 + ch]) * NLOG2E, wav = bflo(we[257 * 128 + ch]) * NLOG2E;
#pragma unroll 1
    for (int eb = 0; eb < EE; eb += 64) {
      const int dl = dst[eb + lane] - n0;
      unsigned long long mk = __builtin_amdgcn_ballot_w64((unsigned)dl < 5u);
      while (mk) {
        const int l = __builtin_ctzll(mk); mk &= mk - 1;
        const int e = eb + l, nw = __builtin_amdgcn_readlane(dl, l), s = src[e], d = n0 + nw;
        u32 w2, dz, d2;
        geom(pos[3 * d] | ((u32)pos[3 * d + 1] << 16), pos[3 * d + 2], pos[3 * s] | ((u32)pos[3 * s + 1] << 16), pos[3 * s + 2], w2, dz, d2);
        const float tb = bflo(((const u16*)(ws + WS_BT))[(s + 1) * 128 + ch]) + bflo(d2) * wdv + bflo(ea[e]) * wav;
#pragma unroll
        for (int i = 0; i < 5; ++i)
          if (nw == i) {
            const float m = bflo(silu2(av[i] + tb, 0.f));
            acc[i][0] += m; acc[i][1] += m * bflo(w2); acc[i][2] += m * bfhi(w2); acc[i][3] += m * bflo(dz);
          }
      }
    }
    const float wxs = bflo(wx[ch]) * NLN2;
#pragma unroll
    for (int i = 0; i < 5; ++i) {
      *LDSP(u16, stg + i * MP + lane * 2) = (u16)pk2(acc[i][0] * NLN2, 0.f);
#pragma unroll
      for (int k = 1; k < 4; ++k) {
        float v = acc[i][k] * wxs;
        v += __shfl_xor(v, 32);
        if (hh == (i & 1)) *LDSP(float, stg + PART + ((i >> 1) * 3 + k - 1) * 256 + lane * 4) = v;
      }
    }
  }
  const int t2 = w * 64 + __builtin_amdgcn_mbcnt_hi(~0u, __builtin_amdgcn_mbcnt_lo(~0u, 0u));
  const u4 hv2 = ld16(rh, t2 * 16);
  const __amdgpu_buffer_rsrc_t rw = mkrsrc(ws + WS_WT + 2 * 32768, w >= 8 ? 32768 : 0);
  u4 wf2[4];
#pragma unroll
  for (int ks = 0; ks < 4; ++ks) wf2[ks] = ld16(rw, ((w & 7) * 4 + ks) * 1024 + lane * 16);
  const u32 pv = pos[(b * NPB + 5 * (w & 7)) * 3 + min(lane >> 2, 14)];
  asm volatile("s_waitcnt lgkmcnt(0)" ::: "memory");
  __builtin_amdgcn_s_barrier();
  asm volatile("" ::: "memory");
  STAMP(st, 11);
  if (w >= 8) {
#pragma unroll
    for (int rt = 0; rt < 3; ++rt) {
      const int n = min(16 * rt + c16, NPB - 1), nwin = (n * 205) >> 10, nw = n - 5 * nwin;
      const u32 pa = L2_PARK + ((w - 8) * NPB + n) * 32 + q4 * 8;
      const u2 pk = *LDSP(u2, pa);
      f4 acc = {bflo(pk[0]), bfhi(pk[0]), bflo(pk[1]), bfhi(pk[1])};
      const u32 ma = L2_STG + nwin * 2 * STG + nw * MP + q4 * 16;
#pragma unroll
      for (int ks = 0; ks < 4; ++ks) acc = MF16(__builtin_bit_cast(s8, wf2[ks]), *LDSP(s8, ma + (ks >> 1) * STG + (ks & 1) * 64), acc);
      f4 o;
#pragma unroll
      for (int j = 0; j < 4; ++j) o[j] = acc[j] * __builtin_amdgcn_rcpf(1.0f + __builtin_amdgcn_exp2f(acc[j] * NLOG2E));
      u2 ov; ov[0] = pk2(o[0], o[1]); ov[1] = pk2(o[2], o[3]);
      if (16 * rt + c16 < NPB) *LDSP(u2, pa) = ov;
    }
  } else {
    const int o = min(lane >> 2, 14), nw = o / 3, k = o - 3 * nw, u = lane & 3;
    const u32 a = L2_STG + (2 * w + (u >> 1)) * STG + PART + ((nw >> 1) * 3 + k) * 256 + (nw & 1) * 128 + (u & 1) * 64;
    const f4 v = *LDSP(f4, a) + *LDSP(f4, a + 16) + *LDSP(f4, a + 32) + *LDSP(f4, a + 48);
    float s = (v[0] + v[1]) + (v[2] + v[3]);
    s += __shfl_xor(s, 1); s += __shfl_xor(s, 2);
    if (lane < 60 && u == 0) out[NN * 128 + (b * NPB + 5 * w) * 3 + o] = (u16)pk2(bflo(pv) + s, 0.f);
  }
  STAMP(st, 12);
  __syncthreads();
  if (t < 640) {
    const u4 sv = *LDSP(u4, L2_PARK + (((t & 15) >> 1) * NPB + (t >> 4)) * 32 + (t & 1) * 16);
    u4 o;
#pragma unroll
    for (int j = 0; j < 4; ++j) o[j] = pk2(bflo(hv2[j]) + bflo(sv[j]), bfhi(hv2[j]) + bfhi(sv[j]));
    *(u4*)(out + (size_t)b * NPB * 128 + t * 8) = o;
  }
  STAMP(st, 13);
  STAMP_END(st, 14);
}

extern "C" void kernel_launch(void* const* d_in, const int* in_sizes, int n_in, void* d_out, int out_size, void* d_ws, size_t ws_size,
                              hipStream_t stream) {
  const u16* h = (const u16*)d_in[0]; const u16* pos = (const u16*)d_in[1]; const u16* ea = (const u16*)d_in[2];
  const int* src = (const int*)d_in[3]; const int* dst = (const int*)d_in[4];
  const u16* we = (const u16*)d_in[5]; const u16* wx = (const u16*)d_in[6]; const u16* wh = (const u16*)d_in[7];
  (void)hipFuncSetAttribute((const void*)k1, hipFuncAttributeMaxDynamicSharedMemorySize, L1_END);
  (void)hipFuncSetAttribute((const void*)k2, hipFuncAttributeMaxDynamicSharedMemorySize, L2_END);
  k1<<<NG + 3, 1024, L1_END, stream>>>(h, pos, ea, src, dst, we, wh, (char*)d_ws);
  k2<<<NB, 1024, L2_END, stream>>>(h, pos, ea, src, dst, we, wx, (char*)d_ws, (u16*)d_out);
}
```

```cpp
#include <hip/hip_runtime.h>
#include <stdint.h>

typedef unsigned short u16;
typedef unsigned int u32;
typedef __attribute__((ext_vector_type(8))) short s8;
typedef __attribute__((ext_vector_type(4))) short s4;
typedef __attribute__((ext_vector_type(2))) float f2;
typedef __attribute__((ext_vector_type(4))) float f4;
typedef __attribute__((ext_vector_type(16))) float f16v;
typedef __attribute__((ext_vector_type(2))) __bf16 bf2;
typedef __attribute__((ext_vector_type(2))) u32 u2;
typedef __attribute__((ext_vector_type(4))) u32 u4;
typedef __attribute__((ext_vector_type(16))) u32 u16v;
typedef __attribute__((address_space(1))) u32 gu32;
typedef __amdgpu_buffer_rsrc_t rsrc_t;
#define LDSP(T, a) ((__attribute__((address_space(3))) T*)(size_t)(a))
#define DI __device__ __forceinline__
#define SB() __builtin_amdgcn_sched_barrier(0)

constexpr int NN = 10000, EE = 320000;
constexpr int NG = 256, NB = 257, NPB = 40, CH = 1250;
constexpr float NLOG2E = -1.4426950408889634f, NLN2 = -0.6931471805599453f;
constexpr u32 WS_EP = 0, WS_FL = 4096, FLS = 256;
constexpr u32 WS_WT = WS_FL + 256 * FLS;
constexpr u32 WS_BT = WS_WT + 65536;
constexpr u32 WS_S1 = WS_BT + 2564096;
constexpr u32 WS_S2 = WS_S1 + 128 * NB * 128;
constexpr u32 WS_END = WS_S2 + 128 * NB * 256;
constexpr u32 OOB = 0x7ffffff0u;
constexpr int WCAP = 224, SP = 456, SBLK = 10496, WXO = 4608, STG = 8320;
constexpr int L_S = 8 * STG, L_H = L_S + 8 * SBLK, L_WDA = L_H + 10240, L_WX = L_WDA + 512, L_PD = L_WX + 256,
              L_WCNT = L_PD + 640, L_FLAG = L_WCNT + 32, L_ZERO = L_FLAG + 16, L_DUM = L_ZERO + 592, L_END = L_DUM + 256;
constexpr int L_CNT = 0, L_IMG = 1280, L_W2 = L_S + WXO, L_W1 = L_S, L_POS = 16, L_TAB = 50688;
constexpr int PART = 2304;
constexpr int mplane(int q) { return q * 512 + (q >> 1) * 64; }
static_assert(L_END <= 163840 && L_S == 66560 && L_H == 150528 && L_WCNT % 32 == 0, "lds");

struct Tab { u32 v[14][64][4]; };
constexpr Tab make_tab() {
  Tab T = {};
  for (int lane = 0; lane < 64; ++lane) {
    const int c32 = lane & 31, hh = lane >> 5, c16 = lane & 15, q4 = lane >> 4;
    u32 c[56] = {};
    const int k0 = c32 - 8 * hh, k1 = k0 - 16;
    const u32 one = (c32 & 1) ? 0x3f800000u : 0x3f80u;
    for (int j = 0; j < 4; ++j) { c[j] = k0 >= 0 && (k0 >> 1) == j ? one : 0u; c[4 + j] = k1 >= 0 && (k1 >> 1) == j ? one : 0u; }
    const u32 gq = (c32 & 3) * 1040 + (c32 >> 2) * 128 + hh * 16;
    c[8] = gq + ((c32 >> 4) << 6); c[9] = gq + (((c32 >> 4) ^ 1) << 6);
    c[10] = ((4 * hh + ((lane >> 2) & 3)) % 7) * SP + (q4 & 1) * 32 + (lane & 3) * 8;
    c[11] = (c32 < 20 ? ((c32 & 3) == 0 ? 2 + (c32 >> 2) : 7 + 3 * (c32 >> 2) + (c32 & 3)) * SP : L_ZERO) + hh * 8;
    c[12] = 7 * SP + (lane >> 3) * 8;
    c[13] = ((lane & 7) ^ (hh << 2)) << 4;
    c[14] = lane * 16; c[15] = lane * 2;
    u32* t = c + 16;
    t[0] = WS_S1 + (lane >> 2) * 128 + (lane & 3) * 16; t[1] = WS_S2 + (lane >> 2) * 128 + (lane & 3) * 16;
    t[2] = 2 * (lane & 3); t[3] = 2 * (lane & 3) + 1; t[4] = 2 * (lane & 3) + 8; t[5] = L_WCNT;
    t[6] = L_WCNT + (lane >> 3) * 4; t[7] = L_WCNT + (lane & 7) * 4;
    t[8] = L_S + (lane >> 3) * (SBLK - 1); t[9] = L_S + (lane & 7) * (SBLK - 1); t[10] = (lane & 7) * 8;
    t[11] = c32 < 20 ? 1 : 0; t[12] = WS_FL + (c16 + 128 * (q4 & 1)) * FLS; t[13] = lane;
    t[14] = L_S + (q4 >> 1) * (SBLK + 16) + WXO + c16 * 16 + (q4 & 1) * 512; t[15] = (lane >> 3) * 64;
    u32* e = c + 32;
    for (int rt = 0; rt < 3; ++rt) {
      const int n = 16 * rt + c16 < NPB ? 16 * rt + c16 : NPB - 1;
      e[rt] = L_H + n * 256 + ((q4 ^ (n & 15)) << 4);
      e[3 + rt] = L_H + n * 256 + (((q4 >> 1) ^ (n & 15)) << 4) + (q4 & 1) * 8;
      e[6 + rt] = L_S + (n / 5) * SBLK + ((n / 5 * 144) & 255) + (n % 5) * 80 + mplane(q4);
    }
    const int o = lane >> 2 < 14 ? lane >> 2 : 14, nw = o / 3, k = o - 3 * nw, u = lane & 3;
    e[9] = L_S + PART + ((nw >> 1) * 3 + k) * 256 + (nw & 1) * 128 + u * 32;
    e[10] = L_PD + nw * 16 + k * 4;
    e[11] = u == 0 && lane < 60 ? o * 2 : OOB;
    e[12] = L_H + lane * 16;
    e[13] = q4 * 256 + ((c16 ^ q4) << 4);
    e[14] = hh * 80 + mplane(c32 >> 3) + (c32 & 7) * 2;
    e[15] = lane * 4;
    for (int rt = 0; rt < 4; ++rt) {
      const int j = c16 & 7, n = 10 * rt + 5 * (c16 >> 3) + j - 2;
      c[48 + rt] = j >= 2 && j < 7 ? L_H + n * 256 + ((q4 ^ (n & 15)) << 4) : ((L_ZERO + 255) & ~255) + q4 * 16;
    }
    c[52] = L_W1 + (8 * q4 + (c16 >> 2)) * 64 + ((((c16 & 3) >> 1) ^ (2 * (q4 & 1))) << 4) + (c16 & 1) * 8;
    c[53] = L_WDA + c16 * 2; c[54] = (q4 & 1) ? 0u : 0xffffu; c[55] = L_WX + c32 * 2;
    for (int i = 0; i < 56; ++i) T.v[i >> 2][lane][i & 3] = c[i];
  }
  return T;
}
static __device__ const __attribute__((aligned(256))) Tab g_tab = make_tab();

DI float bflo(u32 w) { return __uint_as_float(w << 16); }
DI float bfhi(u32 w) { return __uint_as_float(w & 0xffff0000u); }
DI u32 pk2(float a, float b) { f2 v; v.x = a; v.y = b; return __builtin_bit_cast(u32, __builtin_convertvector(v, bf2)); }
DI rsrc_t mkrsrc(const void* p, int bytes) { return __builtin_amdgcn_make_buffer_rsrc((void*)p, 0, bytes, 0x00020000); }
DI void gdma(const void* p, u32 lds, u32 voff) {
  asm volatile("s_mov_b32 m0, %0\n\ts_nop 0\n\tglobal_load_lds_dwordx4 %1, %2" ::"s"(lds), "v"(voff), "s"(p) : "memory");
}
#define WAITV(n) asm volatile("s_waitcnt vmcnt(" #n ")" ::: "memory")
#define BAR() do { asm volatile("s_waitcnt lgkmcnt(0)" ::: "memory"); __builtin_amdgcn_s_barrier(); asm volatile("" ::: "memory"); } while (0)
DI s8 trfrag(u32 a0, u32 a1) {
  s4 lo = __builtin_amdgcn_ds_read_tr16_b64_v4i16(LDSP(s4, a0));
  s4 hi = __builtin_amdgcn_ds_read_tr16_b64_v4i16(LDSP(s4, a1));
  return __builtin_shufflevector(lo, hi, 0, 1, 2, 3, 4, 5, 6, 7);
}
DI u32 mulhi24(u32 a, u32 b) { u32 r; asm("v_mul_hi_u32_u24 %0, %1, %2" : "=v"(r) : "s"(b), "v"(a)); return r; }
DI u32 ldsadd(u32 a) { return __hip_atomic_fetch_add(LDSP(u32, a), 1u, __ATOMIC_RELAXED, __HIP_MEMORY_SCOPE_WORKGROUP); }
#define MF16(a, b, c) __builtin_amdgcn_mfma_f32_16x16x32_bf16((a), (b), (c), 0, 0, 0)

#define PIN(x) asm volatile("" : "+v"(x))
DI float dot2(u32 a, float c) { return __builtin_amdgcn_fdot2_f32_bf16(__builtin_bit_cast(bf2, a), __builtin_bit_cast(bf2, a), c, false); }
DI void geom(float px, float py, float pz, u32 sxy, u32 sz_, u32& w2, u32& dz, u32& d2) {
  w2 = pk2(px - bflo(sxy), py - bfhi(sxy));
  dz = pk2(pz - bflo(sz_), 0.f);
  d2 = pk2(dot2(w2, dot2(dz, 0.f)), 0.f);
}

#define CONS_ASM \
  "s_movk_i32 s99, 0x4000\ns_cmp_lt_u32 %[rt], 4\ns_cbranch_scc1 .Lni%=\nv_mov_b32 v242, 0\nv_mov_b32 v243, 0\nv_mov_b32 v244, 0\nv_mov_b32 v245, 0\n" \
  "v_mov_b32 v246, 0\nv_mov_b32 v247, 0\nv_mov_b32 v248, 0\nv_mov_b32 v249, 0\nv_mov_b32 v128, 0\nv_mov_b32 v129, 0\nv_mov_b32 v130, 0\n" \
  "v_mov_b32 v131, 0\nv_mov_b32 v132, 0\nv_mov_b32 v133, 0\nv_mov_b32 v134, 0\nv_mov_b32 v135, 0\nv_mov_b32 v240, 0x3f80\nv_mov_b32 v241, 0\n" \
  "v_mov_b32 v152, 16\nv_mov_b32 v153, 0x3f80\nv_mfma_f32_32x32x16_bf16 v[0:15], v[242:245], v[242:245], 0\n" \
  "v_mfma_f32_32x32x16_bf16 v[16:31], v[242:245], v[242:245], 0\nv_mfma_f32_32x32x16_bf16 v[32:47], v[242:245], v[242:245], 0\n" \
  "v_mfma_f32_32x32x16_bf16 v[48:63], v[242:245], v[242:245], 0\ns_mov_b32 s82, 0x0c050400\ns_mov_b32 s83, 0x0c070600\ns_mov_b32 s93, 8\n" \
  "s_movk_i32 s97, 456\ns_movk_i32 s98, 1368\ns_mov_b32 s94, 1048576\ns_movk_i32 s84, 4096\ns_movk_i32 s85, 3056\ns_movk_i32 s86, 2016\n" \
  "s_movk_i32 s87, 976\ns_movk_i32 s88, 4224\ns_movk_i32 s89, 3184\ns_movk_i32 s90, 2144\ns_movk_i32 s91, 1104\nv_mov_b32 v155, 6\n" \
  "s_and_b32 s92, %[ep], 0xffff\ns_lshl_b32 s92, s92, 8\ns_or_b32 s92, s92, %[e8]\ns_mov_b32 s69, 0x0c030205\ns_mov_b32 s95, 0x07060302\n" \
  "s_sub_u32 s70, 7, %[rt]\ns_mul_i32 s70, s70, 12288\ns_add_u32 s70, s70, 0\ns_add_u32 m0, s70, 16\ns_nop 0\n" \
  "buffer_load_dwordx4 v150, %[rp], s70 offen offset:0 lds\nbuffer_load_dwordx4 v150, %[rp], s70 offen offset:1024 lds\n" \
  "buffer_load_dwordx4 v150, %[rp], s70 offen offset:2048 lds\nbuffer_load_dwordx4 v150, %[rp], s70 offen offset:3072 lds\ns_add_u32 s70, s70, 4096\n" \
  "s_add_u32 m0, s70, 16\ns_nop 0\nbuffer_load_dwordx4 v150, %[rp], s70 offen offset:0 lds\nbuffer_load_dwordx4 v150, %[rp], s70 offen offset:1024 lds\n" \
  "buffer_load_dwordx4 v150, %[rp], s70 offen offset:2048 lds\nbuffer_load_dwordx4 v150, %[rp], s70 offen offset:3072 lds\ns_add_u32 s70, s70, 4096\n" \
  "s_add_u32 m0, s70, 16\ns_nop 0\nbuffer_load_dwordx4 v150, %[rp], s70 offen offset:0 lds\nbuffer_load_dwordx4 v150, %[rp], s70 offen offset:1024 lds\n" \
  "buffer_load_dwordx4 v150, %[rp], s70 offen offset:2048 lds\nbuffer_load_dwordx4 v150, %[rp], s70 offen offset:3072 lds\ns_add_u32 s70, s70, 4096\n" \
  "s_mul_i32 s70, %[rt], 3072\ns_add_u32 s70, s70, 36864\ns_add_u32 m0, s70, 16\ns_setprio 0\ns_waitcnt vmcnt(12) lgkmcnt(0)\ns_barrier\n" \
  "buffer_load_dwordx4 v150, %[rp], s70 offen offset:0 lds\nbuffer_load_dwordx4 v150, %[rp], s70 offen offset:1024 lds\n" \
  "buffer_load_dwordx4 v150, %[rp], s70 offen offset:2048 lds\nbuffer_load_dwordx4 v[158:161], v218, %[rw], 0 offen sc1\n" \
  "buffer_load_dwordx4 v[162:165], v218, %[rw], s99 offen sc1\ns_mul_i32 s70, %[rt], 10496\ns_add_u32 s70, s70, 31744\nv_add_u32 v234, s70, v150\n" \
  "ds_write_b128 v234, v[242:245] offset:0\nds_write_b128 v234, v[242:245] offset:1024\nds_write_b128 v234, v[242:245] offset:41984\n" \
  "ds_write_b128 v234, v[242:245] offset:43008\ns_branch .Lpoll%=\n.Lni%=:\nv_mov_b32 v234, %[ep]\nv_mov_b32 v235, %[ep]\nv_mov_b32 v236, %[ep]\n" \
  "v_mov_b32 v237, %[ep]\ns_waitcnt vmcnt(0) lgkmcnt(0)\ns_barrier\ns_cmp_lg_u32 %[rt], 0\ns_cbranch_scc1 .Lnf%=\ns_mov_b64 exec, 255\n" \
  "buffer_store_dwordx4 v[234:237], v150, %[rw], %[go] offen sc1\ns_mov_b64 exec, -1\n.Lnf%=:\nbuffer_load_dwordx4 v[158:161], v218, %[rw], 0 offen sc1\n" \
  "buffer_load_dwordx4 v[162:165], v218, %[rw], s99 offen sc1\ns_mov_b32 s70, 0xbfb8aa3b\ns_mov_b32 s71, s70\n" \
  "v_pk_mul_f32 v[166:167], v[166:167], s[70:71]\nv_pk_mul_f32 v[168:169], v[168:169], s[70:71]\nv_pk_mul_f32 v[170:171], v[170:171], s[70:71]\n" \
  "v_pk_mul_f32 v[172:173], v[172:173], s[70:71]\nv_pk_mul_f32 v[174:175], v[174:175], s[70:71]\nv_pk_mul_f32 v[176:177], v[176:177], s[70:71]\n" \
  "v_pk_mul_f32 v[178:179], v[178:179], s[70:71]\nv_pk_mul_f32 v[180:181], v[180:181], s[70:71]\nv_pk_mul_f32 v[182:183], v[182:183], s[70:71]\n" \
  "v_pk_mul_f32 v[184:185], v[184:185], s[70:71]\nv_pk_mul_f32 v[186:187], v[186:187], s[70:71]\nv_pk_mul_f32 v[188:189], v[188:189], s[70:71]\n" \
  "v_pk_mul_f32 v[190:191], v[190:191], s[70:71]\nv_pk_mul_f32 v[192:193], v[192:193], s[70:71]\nv_pk_mul_f32 v[194:195], v[194:195], s[70:71]\n" \
  "v_pk_mul_f32 v[196:197], v[196:197], s[70:71]\nv_cvt_pk_bf16_f32 v166, v166, v167\nv_cvt_pk_bf16_f32 v167, v168, v169\n" \
  "ds_write_b64 v232, v[166:167] offset:0\nv_cvt_pk_bf16_f32 v170, v170, v171\nv_cvt_pk_bf16_f32 v171, v172, v173\n" \
  "ds_write_b64 v232, v[170:171] offset:256\nv_cvt_pk_bf16_f32 v174, v174, v175\nv_cvt_pk_bf16_f32 v175, v176, v177\n" \
  "ds_write_b64 v232, v[174:175] offset:21024\nv_cvt_pk_bf16_f32 v178, v178, v179\nv_cvt_pk_bf16_f32 v179, v180, v181\n" \
  "ds_write_b64 v232, v[178:179] offset:21280\nv_cvt_pk_bf16_f32 v182, v182, v183\nv_cvt_pk_bf16_f32 v183, v184, v185\n" \
  "ds_write_b64 v232, v[182:183] offset:42048\nv_cvt_pk_bf16_f32 v186, v186, v187\nv_cvt_pk_bf16_f32 v187, v188, v189\n" \
  "ds_write_b64 v232, v[186:187] offset:42304\nv_cvt_pk_bf16_f32 v190, v190, v191\nv_cvt_pk_bf16_f32 v191, v192, v193\n" \
  "ds_write_b64 v232, v[190:191] offset:63072\nv_cvt_pk_bf16_f32 v194, v194, v195\nv_cvt_pk_bf16_f32 v195, v196, v197\n" \
  "ds_write_b64 v232, v[194:195] offset:63328\nv_mov_b32 v242, 0\nv_mov_b32 v243, 0\nv_mov_b32 v244, 0\nv_mov_b32 v245, 0\nv_mov_b32 v246, 0\n" \
  "v_mov_b32 v247, 0\nv_mov_b32 v248, 0\nv_mov_b32 v249, 0\nv_mov_b32 v128, 0\nv_mov_b32 v129, 0\nv_mov_b32 v130, 0\nv_mov_b32 v131, 0\n" \
  "v_mov_b32 v132, 0\nv_mov_b32 v133, 0\nv_mov_b32 v134, 0\nv_mov_b32 v135, 0\nv_mov_b32 v240, 0x3f80\nv_mov_b32 v241, 0\nv_mov_b32 v152, 16\n" \
  "v_mov_b32 v153, 0x3f80\nv_mfma_f32_32x32x16_bf16 v[0:15], v[242:245], v[242:245], 0\nv_mfma_f32_32x32x16_bf16 v[16:31], v[242:245], v[242:245], 0\n" \
  "v_mfma_f32_32x32x16_bf16 v[32:47], v[242:245], v[242:245], 0\nv_mfma_f32_32x32x16_bf16 v[48:63], v[242:245], v[242:245], 0\n" \
  "s_mov_b32 s82, 0x0c050400\ns_mov_b32 s83, 0x0c070600\ns_mov_b32 s93, 8\ns_movk_i32 s97, 456\ns_movk_i32 s98, 1368\ns_mov_b32 s94, 1048576\n" \
  "s_movk_i32 s84, 4096\ns_movk_i32 s85, 3056\ns_movk_i32 s86, 2016\ns_movk_i32 s87, 976\ns_movk_i32 s88, 4224\ns_movk_i32 s89, 3184\n" \
  "s_movk_i32 s90, 2144\ns_movk_i32 s91, 1104\nv_mov_b32 v155, 6\ns_and_b32 s92, %[ep], 0xffff\ns_lshl_b32 s92, s92, 8\ns_or_b32 s92, s92, %[e8]\n" \
  "s_mov_b32 s69, 0x0c030205\ns_mov_b32 s95, 0x07060302\n.Lpoll%=:\ns_waitcnt vmcnt(0)\nv_perm_b32 v234, v160, v158, s95\n" \
  "v_perm_b32 v235, v164, v162, s95\nv_bfe_u32 v236, v161, 8, 8\nv_bfe_u32 v237, v165, 8, 8\nv_cmp_ne_u32 s[70:71], %[ep], v234\n" \
  "v_cmp_ne_u32 s[72:73], %[ep], v235\nv_cmp_ne_u32 vcc, %[e8], v236\nv_cmp_ne_u32 s[76:77], %[e8], v237\ns_or_b64 s[70:71], s[70:71], vcc\n" \
  "s_or_b64 s[72:73], s[72:73], s[76:77]\ns_or_b64 vcc, s[70:71], s[72:73]\ns_cbranch_scc0 .Lgo%=\ns_sleep 4\ns_mov_b64 exec, s[70:71]\n" \
  "buffer_load_dwordx4 v[158:161], v218, %[rw], 0 offen sc1\ns_mov_b64 exec, s[72:73]\nbuffer_load_dwordx4 v[162:165], v218, %[rw], s99 offen sc1\n" \
  "s_mov_b64 exec, -1\ns_sub_u32 s94, s94, 1\ns_cbranch_scc0 .Lpoll%=\ns_waitcnt vmcnt(0)\nv_mov_b32 v234, 3\nds_or_b32 v223, v234 offset:32\ns_barrier\n" \
  "s_branch .Lback%=\n.Lgo%=:\nv_bfe_u32 v170, v159, 8, s93\nv_bfe_u32 v171, v163, 8, s93\nv_cmp_lt_u32 s[78:79], v222, v170\n" \
  "v_cmp_lt_u32 s[80:81], v222, v171\nv_cmp_lt_u32 s[70:71], v220, v170\nv_and_or_b32 v172, v159, 28, v223\n" \
  "v_mul_u32_u24_sdwa v178, v155, v158 dst_sel:DWORD dst_unused:UNUSED_PAD src0_sel:DWORD src1_sel:WORD_0\nv_and_b32 v178, -4, v178\n" \
  "s_mov_b64 exec, s[78:79]\nbuffer_load_dwordx4 v[190:193], v219, %[rw], 0 offen sc1\ns_mov_b64 exec, s[80:81]\n" \
  "buffer_load_dwordx4 v[194:197], v219, %[rw], s99 offen sc1\ns_mov_b64 exec, -1\nv_cmp_lt_u32 s[72:73], v220, v171\nv_and_or_b32 v173, v163, 28, v223\n" \
  "v_mul_u32_u24_sdwa v179, v155, v162 dst_sel:DWORD dst_unused:UNUSED_PAD src0_sel:DWORD src1_sel:WORD_0\nv_and_b32 v179, -4, v179\n" \
  "v_cmp_lt_u32 s[74:75], v221, v170\nv_and_or_b32 v174, v161, 28, v223\n" \
  "v_mul_u32_u24_sdwa v180, v155, v160 dst_sel:DWORD dst_unused:UNUSED_PAD src0_sel:DWORD src1_sel:WORD_0\nv_and_b32 v180, -4, v180\n" \
  "v_cmp_lt_u32 s[76:77], v221, v171\nv_and_or_b32 v175, v165, 28, v223\n" \
  "v_mul_u32_u24_sdwa v181, v155, v164 dst_sel:DWORD dst_unused:UNUSED_PAD src0_sel:DWORD src1_sel:WORD_0\nv_and_b32 v181, -4, v181\n" \
  "v_max_u32 v234, v170, v171\ns_mov_b64 exec, s[70:71]\nds_add_rtn_u32 v184, v172, v152\ns_mov_b64 exec, s[72:73]\nds_add_rtn_u32 v185, v173, v152\n" \
  "s_mov_b64 exec, s[74:75]\nds_add_rtn_u32 v186, v174, v152\ns_mov_b64 exec, s[76:77]\nds_add_rtn_u32 v187, v175, v152\ns_barrier\n" \
  "ds_read2_b32 v[212:213], v181 offset0:1 offset1:2\ns_mov_b64 exec, s[74:75]\nds_read2_b32 v[210:211], v180 offset0:1 offset1:2\n" \
  "s_mov_b64 exec, s[72:73]\nds_read2_b32 v[208:209], v179 offset0:1 offset1:2\ns_mov_b64 exec, s[70:71]\n" \
  "ds_read2_b32 v[206:207], v178 offset0:1 offset1:2\ns_mov_b64 exec, -1\nbuffer_load_dword v156, v230, %[rw], 0 offen sc1\ns_mov_b64 exec, s[76:77]\n" \
  "s_waitcnt lgkmcnt(3)\nv_min_u32 v187, 150512, v187\nds_write2_b64 v187, v[164:165], v[212:213] offset1:1\ns_mov_b64 exec, s[74:75]\n" \
  "s_waitcnt lgkmcnt(3)\nv_min_u32 v186, 150512, v186\nds_write2_b64 v186, v[160:161], v[210:211] offset1:1\ns_mov_b64 exec, s[72:73]\n" \
  "s_waitcnt lgkmcnt(3)\nv_min_u32 v185, 150512, v185\nds_write2_b64 v185, v[162:163], v[208:209] offset1:1\ns_mov_b64 exec, s[70:71]\n" \
  "s_waitcnt lgkmcnt(3)\nv_min_u32 v184, 150512, v184\nds_write2_b64 v184, v[158:159], v[206:207] offset1:1\ns_mov_b64 exec, -1\n" \
  "v_add_u32 v235, 1, v222\nv_cmp_lt_u32 s[74:75], v235, v170\nv_cmp_lt_u32 s[76:77], v235, v171\ns_waitcnt vmcnt(1)\n.Lchk%=:\n" \
  "v_perm_b32 v166, v191, v190, s69\nv_perm_b32 v167, v195, v194, s69\nv_perm_b32 v168, v193, v192, s69\nv_perm_b32 v169, v197, v196, s69\n" \
  "v_cmp_ne_u32 s[70:71], s92, v166\nv_cmp_ne_u32 vcc, s92, v168\nv_cmp_ne_u32 s[72:73], s92, v167\ns_and_b64 s[70:71], s[70:71], s[78:79]\n" \
  "s_and_b64 vcc, vcc, s[74:75]\ns_or_b64 s[70:71], s[70:71], vcc\nv_cmp_ne_u32 vcc, s92, v169\ns_and_b64 s[72:73], s[72:73], s[80:81]\n" \
  "s_and_b64 vcc, vcc, s[76:77]\ns_or_b64 s[72:73], s[72:73], vcc\ns_or_b64 vcc, s[70:71], s[72:73]\ns_cbranch_scc1 .Lretry%=\n" \
  "v_and_or_b32 v176, v191, 28, v223\nv_mul_u32_u24_sdwa v182, v155, v190 dst_sel:DWORD dst_unused:UNUSED_PAD src0_sel:DWORD src1_sel:WORD_0\n" \
  "v_and_b32 v182, -4, v182\nv_and_or_b32 v177, v195, 28, v223\n" \
  "v_mul_u32_u24_sdwa v183, v155, v194 dst_sel:DWORD dst_unused:UNUSED_PAD src0_sel:DWORD src1_sel:WORD_0\nv_and_b32 v183, -4, v183\n" \
  "v_and_or_b32 v174, v193, 28, v223\nv_mul_u32_u24_sdwa v180, v155, v192 dst_sel:DWORD dst_unused:UNUSED_PAD src0_sel:DWORD src1_sel:WORD_0\n" \
  "v_and_b32 v180, -4, v180\nv_and_or_b32 v175, v197, 28, v223\n" \
  "v_mul_u32_u24_sdwa v181, v155, v196 dst_sel:DWORD dst_unused:UNUSED_PAD src0_sel:DWORD src1_sel:WORD_0\nv_and_b32 v181, -4, v181\n" \
  "s_mov_b64 exec, s[78:79]\nds_add_rtn_u32 v188, v176, v152\nds_read2_b32 v[214:215], v182 offset0:1 offset1:2\ns_mov_b64 exec, s[80:81]\n" \
  "ds_add_rtn_u32 v189, v177, v152\nds_read2_b32 v[216:217], v183 offset0:1 offset1:2\ns_mov_b64 exec, s[74:75]\nds_add_rtn_u32 v186, v174, v152\n" \
  "ds_read2_b32 v[210:211], v180 offset0:1 offset1:2\ns_mov_b64 exec, s[76:77]\nds_add_rtn_u32 v187, v175, v152\n" \
  "ds_read2_b32 v[212:213], v181 offset0:1 offset1:2\ns_mov_b64 exec, s[78:79]\ns_waitcnt lgkmcnt(6)\nv_min_u32 v188, 150512, v188\n" \
  "ds_write2_b64 v188, v[190:191], v[214:215] offset1:1\ns_mov_b64 exec, s[80:81]\ns_waitcnt lgkmcnt(5)\nv_min_u32 v189, 150512, v189\n" \
  "ds_write2_b64 v189, v[194:195], v[216:217] offset1:1\ns_mov_b64 exec, s[74:75]\ns_waitcnt lgkmcnt(4)\nv_min_u32 v186, 150512, v186\n" \
  "ds_write2_b64 v186, v[192:193], v[210:211] offset1:1\ns_mov_b64 exec, s[76:77]\ns_waitcnt lgkmcnt(3)\nv_min_u32 v187, 150512, v187\n" \
  "ds_write2_b64 v187, v[196:197], v[212:213] offset1:1\ns_mov_b64 exec, -1\n.Lfc%=:\ns_waitcnt vmcnt(0)\nv_cmp_ne_u32 vcc, %[ep], v156\n" \
  "s_cbranch_vccnz .Lfr%=\nv_cmp_lt_u32 vcc, 16, v234\ns_cbranch_vccnz .Lrare%=\n.Lback%=:\ns_waitcnt lgkmcnt(0)\ns_barrier\nds_read_b32 v206, v224\n" \
  "ds_read_b32 v207, v225\nds_read_b32 v208, v223 offset:32\ns_movk_i32 s94, 4623\ns_waitcnt lgkmcnt(1)\nv_sub_u32 v206, v206, v226\n" \
  "v_sub_u32 v207, v207, v227\nv_cmp_gt_u32 vcc, v207, v206\nv_cmp_lt_u32 s[70:71], s94, v207\ns_waitcnt lgkmcnt(0)\nv_readfirstlane_b32 %[st], v208\n" \
  "v_lshrrev_b64 v[234:235], v228, vcc\nv_and_b32 v234, 0xff, v234\nv_bcnt_u32_b32 v234, v234, 0\nv_cmp_eq_u32 vcc, %[rt], v234\n" \
  "s_cmp_lg_u64 s[70:71], 0\ns_cselect_b32 s94, 4, 0\ns_ff1_i32_b64 %[win], vcc\nv_readlane_b32 %[rem], v207, %[win]\ns_mul_i32 s95, %[win], 10496\n" \
  "s_add_u32 s95, s95, 66560\ns_or_b32 %[st], %[st], s94\ns_cbranch_scc1 .Lend%=\ns_add_u32 s94, %[go], %[rt]\ns_cmp_lg_u32 s94, 4103\n" \
  "s_cbranch_scc1 .Lne%=\nv_mov_b32 v234, %[ep]\ns_mov_b64 exec, 1\nbuffer_store_dword v234, v150, %[rw], 0 offen sc1\ns_mov_b64 exec, -1\n.Lne%=:\n" \
  "v_add_u32 v233, s95, v233\nv_add_u32 v154, s95, v150\nds_read2_b32 v[206:207], v233 offset1:4\nds_read2_b32 v[208:209], v233 offset0:8 offset1:12\n" \
  "ds_read_b128 v[158:161], v154 offset:0\nds_read_b128 v[162:165], v154 offset:1024\nds_read_b128 v[166:169], v154 offset:2048\n" \
  "ds_read_b128 v[170:173], v154 offset:3072\nds_read_b128 v[84:87], v154 offset:3584\ns_lshr_b32 s68, %[rem], 4\ns_min_u32 %[rem], s68, 224\n" \
  "s_sub_u32 s68, s68, %[rem]\ns_mul_i32 s96, %[win], 80\ns_add_u32 s96, s96, 161536\ns_mov_b32 m0, %[stg]\nv_add_u32 v146, s95, v146\n" \
  "v_add_u32 v148, s95, v148\nv_add_u32 v151, s95, v151\nv_mad_u32_u24 v147, v229, s95, v147\ns_waitcnt lgkmcnt(5)\nv_perm_b32 v252, v206, v149, s82\n" \
  "v_perm_b32 v253, v207, v149, s82\nv_perm_b32 v254, v208, v149, s82\nv_perm_b32 v255, v209, v149, s82\ns_cmp_gt_u32 %[rt], 3\ns_cbranch_scc1 .Lg1%=\n" \
  "buffer_load_dwordx4 v252, %[rb], s84 offen offset:0 sc1 lds\nbuffer_load_dwordx4 v253, %[rb], s85 offen offset:1040 sc1 lds\n" \
  "buffer_load_dwordx4 v254, %[rb], s86 offen offset:2080 sc1 lds\nbuffer_load_dwordx4 v255, %[rb], s87 offen offset:3120 sc1 lds\n" \
  "s_add_u32 m0, %[stg], 4160\ns_nop 0\nbuffer_load_dwordx4 v252, %[rb], s88 offen offset:0 sc1 lds\n" \
  "buffer_load_dwordx4 v253, %[rb], s89 offen offset:1040 sc1 lds\nbuffer_load_dwordx4 v254, %[rb], s90 offen offset:2080 sc1 lds\n" \
  "buffer_load_dwordx4 v255, %[rb], s91 offen offset:3120 sc1 lds\n.Lg1%=:\ns_waitcnt lgkmcnt(0)\nv_cmp_gt_i32 s[70:71], %[rem], v231\n" \
  "s_sub_i32 s94, %[rem], 64\nv_cmp_gt_i32 s[72:73], s94, v231\ns_sub_i32 s94, %[rem], 128\nv_cmp_gt_i32 s[74:75], s94, v231\n" \
  "s_sub_i32 s94, %[rem], 192\nv_cmp_gt_i32 s[76:77], s94, v231\nv_bfe_u32 v210, v159, 5, 3\nv_lshl_add_u32 v214, v210, 4, s96\n" \
  "ds_read_b128 v[174:177], v214\nv_bfe_u32 v211, v163, 5, 3\nv_lshl_add_u32 v215, v211, 4, s96\nds_read_b128 v[178:181], v215\n" \
  "v_bfe_u32 v212, v167, 5, 3\nv_lshl_add_u32 v216, v212, 4, s96\nds_read_b128 v[182:185], v216\nv_lshl_add_u32 v206, %[win], 4, v154\n" \
  "ds_read_b128 v[120:123], v206 offset:4608\nds_read_b128 v[124:127], v206 offset:5632\nds_write_b128 v154, v[242:245] offset:0\n" \
  "ds_write_b128 v154, v[242:245] offset:1024\nds_write_b128 v154, v[242:245] offset:2048\nds_write_b128 v154, v[242:245] offset:3072\n" \
  "ds_write_b128 v154, v[242:245] offset:4096\nds_write_b128 v154, v[242:245] offset:5120\nds_write_b128 v154, v[242:245] offset:6144\n" \
  "v_lshlrev_b32 v193, 1, v158\nv_alignbyte_b32 v191, v161, v160, v193\nv_alignbyte_b32 v192, 0, v161, v193\nv_lshlrev_b32 v190, 16, v191\n" \
  "v_and_b32 v191, 0xffff0000, v191\nv_lshlrev_b32 v192, 16, v192\nv_mad_u32_u24 v196, v210, s97, v151\nv_mad_u32_u24 v197, v210, s98, v151\n" \
  "v_lshlrev_b32 v221, 1, v162\nv_alignbyte_b32 v219, v165, v164, v221\nv_alignbyte_b32 v220, 0, v165, v221\nv_lshlrev_b32 v218, 16, v219\n" \
  "v_and_b32 v219, 0xffff0000, v219\nv_lshlrev_b32 v220, 16, v220\nv_mad_u32_u24 v224, v211, s97, v151\nv_mad_u32_u24 v225, v211, s98, v151\n" \
  "v_lshlrev_b32 v229, 1, v166\nv_alignbyte_b32 v227, v169, v168, v229\nv_alignbyte_b32 v228, 0, v169, v229\nv_lshlrev_b32 v226, 16, v227\n" \
  "v_and_b32 v227, 0xffff0000, v227\nv_lshlrev_b32 v228, 16, v228\nv_mad_u32_u24 v232, v212, s97, v151\nv_mad_u32_u24 v233, v212, s98, v151\n" \
  "s_waitcnt lgkmcnt(9)\nv_sub_f32 v190, v174, v190\nv_sub_f32 v191, v175, v191\nv_sub_f32 v192, v176, v192\nv_cvt_pk_bf16_f32 v193, v190, v191\n" \
  "v_cvt_pk_bf16_f32 v194, v192, 0\nv_dot2_f32_bf16 v195, v194, v194, 0\nv_dot2_f32_bf16 v195, v193, v193, v195\nv_sub_f32 v218, v178, v218\n" \
  "v_sub_f32 v219, v179, v219\nv_sub_f32 v220, v180, v220\nv_cvt_pk_bf16_f32 v221, v218, v219\nv_cvt_pk_bf16_f32 v222, v220, 0\n" \
  "v_dot2_f32_bf16 v223, v222, v222, 0\nv_dot2_f32_bf16 v223, v221, v221, v223\nv_sub_f32 v226, v182, v226\nv_sub_f32 v227, v183, v227\n" \
  "v_sub_f32 v228, v184, v228\nv_cvt_pk_bf16_f32 v229, v226, v227\nv_cvt_pk_bf16_f32 v230, v228, 0\nv_dot2_f32_bf16 v231, v230, v230, 0\n" \
  "v_dot2_f32_bf16 v231, v229, v229, v231\ns_cmp_lt_u32 %[rt], 4\ns_cbranch_scc1 .Lg2%=\nbuffer_load_dwordx4 v252, %[rb], s84 offen offset:0 sc1 lds\n" \
  "buffer_load_dwordx4 v253, %[rb], s85 offen offset:1040 sc1 lds\nbuffer_load_dwordx4 v254, %[rb], s86 offen offset:2080 sc1 lds\n" \
  "buffer_load_dwordx4 v255, %[rb], s87 offen offset:3120 sc1 lds\ns_add_u32 m0, %[stg], 4160\ns_nop 0\n" \
  "buffer_load_dwordx4 v252, %[rb], s88 offen offset:0 sc1 lds\nbuffer_load_dwordx4 v253, %[rb], s89 offen offset:1040 sc1 lds\n" \
  "buffer_load_dwordx4 v254, %[rb], s90 offen offset:2080 sc1 lds\nbuffer_load_dwordx4 v255, %[rb], s91 offen offset:3120 sc1 lds\n.Lg2%=:\n" \
  "s_mov_b64 exec, s[70:71]\nds_write_b16 v151, v158 offset:3192\nds_write_b16 v196, v153 offset:912\nds_write_b16_d16_hi v151, v159 offset:456\n" \
  "v_cvt_pk_bf16_f32 v195, v195, 0\nds_write_b16 v197, v193 offset:3648\nds_write_b16_d16_hi v197, v193 offset:4104\n" \
  "ds_write_b16 v197, v194 offset:4560\nds_write_b16 v151, v195 offset:0\ns_mov_b64 exec, s[72:73]\nds_write_b16 v151, v162 offset:3320\n" \
  "ds_write_b16 v224, v153 offset:1040\nds_write_b16_d16_hi v151, v163 offset:584\nv_cvt_pk_bf16_f32 v223, v223, 0\nds_write_b16 v225, v221 offset:3776\n" \
  "ds_write_b16_d16_hi v225, v221 offset:4232\nds_write_b16 v225, v222 offset:4688\nds_write_b16 v151, v223 offset:128\ns_mov_b64 exec, s[74:75]\n" \
  "ds_write_b16 v151, v166 offset:3448\nds_write_b16 v232, v153 offset:1168\nds_write_b16_d16_hi v151, v167 offset:712\nv_cvt_pk_bf16_f32 v231, v231, 0\n" \
  "ds_write_b16 v233, v229 offset:3904\nds_write_b16_d16_hi v233, v229 offset:4360\nds_write_b16 v233, v230 offset:4816\n" \
  "ds_write_b16 v151, v231 offset:256\ns_mov_b64 exec, -1\ns_cmp_gt_i32 %[rem], 192\ns_cbranch_scc1 .Lr4%=\n.Lr4b%=:\ns_setprio 3\ns_waitcnt vmcnt(0)\n" \
  "ds_read_b64_tr_b16 v[238:239], v146\nds_read_b128 v[206:209], v144 offset:0\nds_read_b128 v[210:213], v144 offset:32\n" \
  "ds_read_b128 v[214:217], v145 offset:0\nds_read_b128 v[218:221], v145 offset:32\nds_read_b128 v[222:225], v144 offset:4160\n" \
  "ds_read_b128 v[226:229], v144 offset:4192\nds_read_b128 v[230:233], v145 offset:4160\nds_read_b128 v[234:237], v145 offset:4192\n" \
  "ds_read_b64 v[250:251], v148 offset:64\nv_mfma_f32_32x32x16_bf16 v[174:189], v[242:245], v[242:245], 0\ns_mov_b32 m0, %[stg]\ns_waitcnt lgkmcnt(0)\n" \
  "v_mfma_f32_32x32x16_bf16 v[158:173], v[206:209], v[136:139], 0\nv_mfma_f32_32x32x16_bf16 v[158:173], v[210:213], v[140:143], v[158:173]\n" \
  "v_mfma_f32_32x32x8_bf16 v[158:173], v[238:239], v[120:121], v[158:173]\nv_perm_b32 v252, v250, v149, s82\nv_perm_b32 v253, v250, v149, s83\n" \
  "v_perm_b32 v254, v251, v149, s82\nv_perm_b32 v255, v251, v149, s83\nbuffer_load_dwordx4 v252, %[rb], s84 offen offset:0 sc1 lds\n" \
  "buffer_load_dwordx4 v253, %[rb], s85 offen offset:1040 sc1 lds\nbuffer_load_dwordx4 v254, %[rb], s86 offen offset:2080 sc1 lds\n" \
  "buffer_load_dwordx4 v255, %[rb], s87 offen offset:3120 sc1 lds\ns_add_u32 m0, %[stg], 4160\ns_nop 0\n" \
  "buffer_load_dwordx4 v252, %[rb], s88 offen offset:0 sc1 lds\nbuffer_load_dwordx4 v253, %[rb], s89 offen offset:1040 sc1 lds\n" \
  "buffer_load_dwordx4 v254, %[rb], s90 offen offset:2080 sc1 lds\nbuffer_load_dwordx4 v255, %[rb], s91 offen offset:3120 sc1 lds\n.Ltop%=:\n" \
  "s_cmp_gt_i32 %[rem], 56\ns_cbranch_scc1 .Lloop%=\nbuffer_load_dwordx4 v[104:107], v150, %[rw], %[fp] offen offset:0 sc1\n" \
  "buffer_load_dwordx4 v[108:111], v150, %[rw], %[fp] offen offset:1024 sc1\nbuffer_load_dwordx4 v[112:115], v150, %[rw], %[fp] offen offset:2048 sc1\n" \
  "buffer_load_dwordx4 v[116:119], v150, %[rw], %[fp] offen offset:3072 sc1\ns_add_u32 s94, %[fp], 4096\n" \
  "buffer_load_dwordx4 v[88:91], v150, %[rw], s94 offen offset:0 sc1\nbuffer_load_dwordx4 v[92:95], v150, %[rw], s94 offen offset:1024 sc1\n" \
  "buffer_load_dwordx4 v[96:99], v150, %[rw], s94 offen offset:2048 sc1\nbuffer_load_dwordx4 v[100:103], v150, %[rw], s94 offen offset:3072 sc1\n" \
  "s_cmp_lt_i32 %[rem], 25\ns_cbranch_scc0 .Lloop%=\ns_waitcnt vmcnt(0)\ns_branch .Ltail%=\n.Lretry%=:\ns_sleep 2\ns_mov_b64 exec, s[70:71]\n" \
  "buffer_load_dwordx4 v[190:193], v219, %[rw], 0 offen sc1\ns_mov_b64 exec, s[72:73]\nbuffer_load_dwordx4 v[194:197], v219, %[rw], s99 offen sc1\n" \
  "s_mov_b64 exec, -1\ns_waitcnt vmcnt(0)\ns_sub_u32 s94, s94, 1\ns_cbranch_scc0 .Lchk%=\ns_branch .Lto%=\n.Lfr%=:\ns_sleep 1\ns_mov_b64 exec, vcc\n" \
  "buffer_load_dword v156, v230, %[rw], 0 offen sc1\ns_mov_b64 exec, -1\ns_sub_u32 s94, s94, 1\ns_cbranch_scc0 .Lfc%=\n.Lto%=:\ns_waitcnt vmcnt(0)\n" \
  "v_mov_b32 v234, 3\nds_or_b32 v223, v234 offset:32\ns_branch .Lback%=\n.Lrare%=:\nv_cmp_lt_u32 vcc, 24, v234\nv_mov_b32 v235, 1\ns_mov_b64 exec, vcc\n" \
  "ds_or_b32 v223, v235 offset:32\ns_mov_b64 exec, -1\nv_add_u32 v219, 32896, v219\nv_add_u32 v222, 8, v222\nv_cmp_lt_u32 s[78:79], v222, v170\n" \
  "v_cmp_lt_u32 s[80:81], v222, v171\nv_add_u32 v235, 1, v222\nv_cmp_lt_u32 s[74:75], v235, v170\nv_cmp_lt_u32 s[76:77], v235, v171\n" \
  "s_mov_b64 exec, s[78:79]\nbuffer_load_dwordx4 v[190:193], v219, %[rw], 0 offen sc1\ns_mov_b64 exec, s[80:81]\n" \
  "buffer_load_dwordx4 v[194:197], v219, %[rw], s99 offen sc1\ns_mov_b64 exec, -1\ns_waitcnt vmcnt(0) lgkmcnt(0)\nv_and_or_b32 v176, v191, 28, v223\n" \
  "v_mul_u32_u24_sdwa v182, v155, v190 dst_sel:DWORD dst_unused:UNUSED_PAD src0_sel:DWORD src1_sel:WORD_0\nv_and_b32 v182, -4, v182\n" \
  "v_and_or_b32 v177, v195, 28, v223\nv_mul_u32_u24_sdwa v183, v155, v194 dst_sel:DWORD dst_unused:UNUSED_PAD src0_sel:DWORD src1_sel:WORD_0\n" \
  "v_and_b32 v183, -4, v183\nv_and_or_b32 v174, v193, 28, v223\n" \
  "v_mul_u32_u24_sdwa v180, v155, v192 dst_sel:DWORD dst_unused:UNUSED_PAD src0_sel:DWORD src1_sel:WORD_0\nv_and_b32 v180, -4, v180\n" \
  "v_and_or_b32 v175, v197, 28, v223\nv_mul_u32_u24_sdwa v181, v155, v196 dst_sel:DWORD dst_unused:UNUSED_PAD src0_sel:DWORD src1_sel:WORD_0\n" \
  "v_and_b32 v181, -4, v181\ns_mov_b64 exec, s[78:79]\nds_add_rtn_u32 v188, v176, v152\nds_read2_b32 v[214:215], v182 offset0:1 offset1:2\n" \
  "s_mov_b64 exec, s[80:81]\nds_add_rtn_u32 v189, v177, v152\nds_read2_b32 v[216:217], v183 offset0:1 offset1:2\ns_mov_b64 exec, s[74:75]\n" \
  "ds_add_rtn_u32 v186, v174, v152\nds_read2_b32 v[210:211], v180 offset0:1 offset1:2\ns_mov_b64 exec, s[76:77]\nds_add_rtn_u32 v187, v175, v152\n" \
  "ds_read2_b32 v[212:213], v181 offset0:1 offset1:2\ns_mov_b64 exec, s[78:79]\ns_waitcnt lgkmcnt(6)\nv_min_u32 v188, 150512, v188\n" \
  "ds_write2_b64 v188, v[190:191], v[214:215] offset1:1\ns_mov_b64 exec, s[80:81]\ns_waitcnt lgkmcnt(5)\nv_min_u32 v189, 150512, v189\n" \
  "ds_write2_b64 v189, v[194:195], v[216:217] offset1:1\ns_mov_b64 exec, s[74:75]\ns_waitcnt lgkmcnt(4)\nv_min_u32 v186, 150512, v186\n" \
  "ds_write2_b64 v186, v[192:193], v[210:211] offset1:1\ns_mov_b64 exec, s[76:77]\ns_waitcnt lgkmcnt(3)\nv_min_u32 v187, 150512, v187\n" \
  "ds_write2_b64 v187, v[196:197], v[212:213] offset1:1\ns_mov_b64 exec, -1\ns_branch .Lback%=\n.Lr4%=:\nv_bfe_u32 v213, v171, 5, 3\n" \
  "v_lshl_add_u32 v217, v213, 4, s96\nds_read_b128 v[186:189], v217\nv_lshlrev_b32 v193, 1, v170\nv_alignbyte_b32 v191, v173, v172, v193\n" \
  "v_alignbyte_b32 v192, 0, v173, v193\nv_lshlrev_b32 v190, 16, v191\nv_and_b32 v191, 0xffff0000, v191\nv_lshlrev_b32 v192, 16, v192\n" \
  "v_mad_u32_u24 v196, v213, s97, v151\nv_mad_u32_u24 v197, v213, s98, v151\ns_waitcnt lgkmcnt(0)\nv_sub_f32 v190, v186, v190\n" \
  "v_sub_f32 v191, v187, v191\nv_sub_f32 v192, v188, v192\nv_cvt_pk_bf16_f32 v193, v190, v191\nv_cvt_pk_bf16_f32 v194, v192, 0\n" \
  "v_dot2_f32_bf16 v195, v194, v194, 0\nv_dot2_f32_bf16 v195, v193, v193, v195\ns_mov_b64 exec, s[76:77]\nds_write_b16 v151, v170 offset:3576\n" \
  "ds_write_b16 v196, v153 offset:1296\nds_write_b16_d16_hi v151, v171 offset:840\nv_cvt_pk_bf16_f32 v195, v195, 0\nds_write_b16 v197, v193 offset:4032\n" \
  "ds_write_b16_d16_hi v197, v193 offset:4488\nds_write_b16 v197, v194 offset:4944\nds_write_b16 v151, v195 offset:384\ns_mov_b64 exec, -1\n" \
  "s_branch .Lr4b%=\n.Lp2%=:\nv_mov_b32 v242, 0\nv_mov_b32 v243, 0\nv_mov_b32 v244, 0\nv_mov_b32 v245, 0\nv_mov_b32 v246, 0\nv_mov_b32 v247, 0\n" \
  "v_mov_b32 v248, 0\nv_mov_b32 v249, 0\nv_mov_b32 v128, 0\nv_mov_b32 v129, 0\nv_mov_b32 v130, 0\nv_mov_b32 v131, 0\nv_mov_b32 v132, 0\n" \
  "v_mov_b32 v133, 0\nv_mov_b32 v134, 0\nv_mov_b32 v135, 0\nv_mov_b32 v158, v84\nv_mov_b32 v159, v85\nv_mov_b32 v160, v86\nv_mov_b32 v161, v87\n" \
  "v_subrev_u32 v146, 448, v146\nv_subrev_u32 v148, 448, v148\nv_subrev_u32 v147, 448, v147\nv_lshrrev_b32 v206, 4, v150\ns_mov_b32 %[rem], s68\n" \
  "s_mov_b32 s68, 0\nv_cmp_gt_i32 s[70:71], %[rem], v206\nds_write_b128 v154, v[242:245] offset:0\nds_write_b128 v154, v[242:245] offset:1024\n" \
  "ds_write_b128 v154, v[242:245] offset:2048\nds_write_b128 v154, v[242:245] offset:3072\nds_write_b128 v154, v[242:245] offset:4096\n" \
  "ds_write_b128 v154, v[242:245] offset:5120\nds_write_b128 v154, v[242:245] offset:6144\nds_write_b128 v154, v[242:245] offset:7168\n" \
  "ds_write_b128 v154, v[242:245] offset:8192\nds_write_b128 v154, v[242:245] offset:9216\ns_mov_b64 exec, 0xffff\n" \
  "ds_write_b128 v154, v[242:245] offset:10240\ns_mov_b64 exec, -1\nv_bfe_u32 v210, v159, 5, 3\nv_lshl_add_u32 v214, v210, 4, s96\n" \
  "ds_read_b128 v[174:177], v214\nv_lshlrev_b32 v193, 1, v158\nv_alignbyte_b32 v191, v161, v160, v193\nv_alignbyte_b32 v192, 0, v161, v193\n" \
  "v_lshlrev_b32 v190, 16, v191\nv_and_b32 v191, 0xffff0000, v191\nv_lshlrev_b32 v192, 16, v192\nv_mad_u32_u24 v196, v210, s97, v151\n" \
  "v_mad_u32_u24 v197, v210, s98, v151\ns_waitcnt lgkmcnt(0)\nv_sub_f32 v190, v174, v190\nv_sub_f32 v191, v175, v191\nv_sub_f32 v192, v176, v192\n" \
  "v_cvt_pk_bf16_f32 v193, v190, v191\nv_cvt_pk_bf16_f32 v194, v192, 0\nv_dot2_f32_bf16 v195, v194, v194, 0\nv_dot2_f32_bf16 v195, v193, v193, v195\n" \
  "s_mov_b64 exec, s[70:71]\nds_write_b16 v151, v158 offset:3192\nds_write_b16 v196, v153 offset:912\nds_write_b16_d16_hi v151, v159 offset:456\n" \
  "v_cvt_pk_bf16_f32 v195, v195, 0\nds_write_b16 v197, v193 offset:3648\nds_write_b16_d16_hi v197, v193 offset:4104\n" \
  "ds_write_b16 v197, v194 offset:4560\nds_write_b16 v151, v195 offset:0\ns_mov_b64 exec, -1\nds_read_b64 v[250:251], v148\ns_mov_b32 m0, %[stg]\n" \
  "s_waitcnt vmcnt(0) lgkmcnt(0)\nv_perm_b32 v252, v250, v149, s82\nv_perm_b32 v253, v250, v149, s83\nv_perm_b32 v254, v251, v149, s82\n" \
  "v_perm_b32 v255, v251, v149, s83\nbuffer_load_dwordx4 v252, %[rb], s84 offen offset:0 sc1 lds\n" \
  "buffer_load_dwordx4 v253, %[rb], s85 offen offset:1040 sc1 lds\nbuffer_load_dwordx4 v254, %[rb], s86 offen offset:2080 sc1 lds\n" \
  "buffer_load_dwordx4 v255, %[rb], s87 offen offset:3120 sc1 lds\ns_add_u32 m0, %[stg], 4160\ns_nop 0\n" \
  "buffer_load_dwordx4 v252, %[rb], s88 offen offset:0 sc1 lds\nbuffer_load_dwordx4 v253, %[rb], s89 offen offset:1040 sc1 lds\n" \
  "buffer_load_dwordx4 v254, %[rb], s90 offen offset:2080 sc1 lds\nbuffer_load_dwordx4 v255, %[rb], s91 offen offset:3120 sc1 lds\ns_branch .Lr4b%=\n" \
  ".Lloop%=:\nv_exp_f32 v190, v158\nv_mfma_f32_32x32x16_bf16 v[32:47], v[242:245], v[128:131], v[32:47]\nv_exp_f32 v191, v159\nv_exp_f32 v192, v160\n" \
  "v_exp_f32 v193, v161\nv_mfma_f32_32x32x16_bf16 v[32:47], v[246:249], v[132:135], v[32:47]\nv_exp_f32 v194, v162\nv_exp_f32 v195, v163\n" \
  "v_exp_f32 v196, v164\nv_exp_f32 v197, v165\nv_exp_f32 v198, v166\nv_exp_f32 v199, v167\nv_exp_f32 v200, v168\nv_exp_f32 v201, v169\n" \
  "v_exp_f32 v202, v170\nv_exp_f32 v203, v171\nv_exp_f32 v204, v172\nv_exp_f32 v205, v173\nv_cvt_pk_bf16_f32 v128, v174, v175\n" \
  "v_cvt_pk_bf16_f32 v129, v176, v177\nv_mfma_f32_16x16x4_4b_bf16 v[190:205], v[240:241], v[240:241], v[190:205]\nv_cvt_pk_bf16_f32 v130, v178, v179\n" \
  "v_cvt_pk_bf16_f32 v131, v180, v181\nv_cvt_pk_bf16_f32 v132, v182, v183\nv_cvt_pk_bf16_f32 v133, v184, v185\nv_cvt_pk_bf16_f32 v134, v186, v187\n" \
  "v_cvt_pk_bf16_f32 v135, v188, v189\nv_add_u32 v146, 64, v146\nv_add_u32 v148, 64, v148\ns_sub_i32 %[rem], %[rem], 32\ns_cmp_gt_i32 %[rem], 88\n" \
  "s_cbranch_scc1 .Lp%=\ns_setprio 2\ns_cmp_gt_i32 %[rem], 40\ns_cbranch_scc1 .Lp%=\ns_setprio 1\ns_cmp_gt_i32 %[rem], -8\ns_cbranch_scc1 .Lp%=\n" \
  "s_setprio 0\n.Lp%=:\nv_mfma_f32_32x32x16_bf16 v[174:189], v[214:217], v[136:139], 0\nv_rcp_f32 v190, v190\nv_rcp_f32 v191, v191\n" \
  "v_rcp_f32 v192, v192\nv_mfma_f32_32x32x16_bf16 v[174:189], v[218:221], v[140:143], v[174:189]\nv_rcp_f32 v193, v193\nv_rcp_f32 v194, v194\n" \
  "v_rcp_f32 v195, v195\nv_mfma_f32_32x32x8_bf16 v[174:189], v[238:239], v[122:123], v[174:189]\nv_rcp_f32 v196, v196\nv_rcp_f32 v197, v197\n" \
  "v_rcp_f32 v198, v198\nv_rcp_f32 v199, v199\nv_rcp_f32 v200, v200\nv_rcp_f32 v201, v201\nv_rcp_f32 v202, v202\nv_rcp_f32 v203, v203\n" \
  "v_rcp_f32 v204, v204\nv_rcp_f32 v205, v205\nv_pk_mul_f32 v[158:159], v[158:159], v[190:191]\nv_pk_mul_f32 v[160:161], v[160:161], v[192:193]\n" \
  "v_pk_mul_f32 v[162:163], v[162:163], v[194:195]\nv_pk_mul_f32 v[164:165], v[164:165], v[196:197]\nv_pk_mul_f32 v[166:167], v[166:167], v[198:199]\n" \
  "v_pk_mul_f32 v[168:169], v[168:169], v[200:201]\nv_pk_mul_f32 v[170:171], v[170:171], v[202:203]\nv_pk_mul_f32 v[172:173], v[172:173], v[204:205]\n" \
  "v_exp_f32 v190, v174\nv_mfma_f32_32x32x16_bf16 v[48:63], v[242:245], v[128:131], v[48:63]\nv_exp_f32 v191, v175\nv_exp_f32 v192, v176\n" \
  "v_exp_f32 v193, v177\nv_mfma_f32_32x32x16_bf16 v[48:63], v[246:249], v[132:135], v[48:63]\nv_exp_f32 v194, v178\nv_exp_f32 v195, v179\n" \
  "v_exp_f32 v196, v180\nv_exp_f32 v197, v181\nv_exp_f32 v198, v182\nv_exp_f32 v199, v183\nv_exp_f32 v200, v184\nv_exp_f32 v201, v185\n" \
  "v_exp_f32 v202, v186\nv_exp_f32 v203, v187\nv_exp_f32 v204, v188\nv_exp_f32 v205, v189\nv_cvt_pk_bf16_f32 v128, v158, v159\n" \
  "v_cvt_pk_bf16_f32 v129, v160, v161\nv_mfma_f32_16x16x4_4b_bf16 v[190:205], v[240:241], v[240:241], v[190:205]\nv_cvt_pk_bf16_f32 v130, v162, v163\n" \
  "v_cvt_pk_bf16_f32 v131, v164, v165\nv_cvt_pk_bf16_f32 v132, v166, v167\nv_cvt_pk_bf16_f32 v133, v168, v169\nv_cvt_pk_bf16_f32 v134, v170, v171\n" \
  "v_cvt_pk_bf16_f32 v135, v172, v173\nds_read_b64 v[242:243], v147 offset:0\nds_read_b64 v[244:245], v147 offset:16\n" \
  "ds_read_b64 v[246:247], v147 offset:32\nds_read_b64 v[248:249], v147 offset:48\nv_add_u32 v147, 64, v147\n" \
  "v_mfma_f32_32x32x16_bf16 v[158:173], v[222:225], v[136:139], 0\nv_rcp_f32 v190, v190\nv_rcp_f32 v191, v191\nv_rcp_f32 v192, v192\n" \
  "v_mfma_f32_32x32x16_bf16 v[158:173], v[226:229], v[140:143], v[158:173]\nv_rcp_f32 v193, v193\nv_rcp_f32 v194, v194\nv_rcp_f32 v195, v195\n" \
  "v_mfma_f32_32x32x8_bf16 v[158:173], v[238:239], v[124:125], v[158:173]\nv_rcp_f32 v196, v196\nv_rcp_f32 v197, v197\nv_rcp_f32 v198, v198\n" \
  "v_rcp_f32 v199, v199\nv_rcp_f32 v200, v200\nv_rcp_f32 v201, v201\nv_rcp_f32 v202, v202\nv_rcp_f32 v203, v203\nv_rcp_f32 v204, v204\n" \
  "v_rcp_f32 v205, v205\nv_pk_mul_f32 v[174:175], v[174:175], v[190:191]\nv_pk_mul_f32 v[176:177], v[176:177], v[192:193]\n" \
  "v_pk_mul_f32 v[178:179], v[178:179], v[194:195]\nv_pk_mul_f32 v[180:181], v[180:181], v[196:197]\nv_pk_mul_f32 v[182:183], v[182:183], v[198:199]\n" \
  "v_pk_mul_f32 v[184:185], v[184:185], v[200:201]\nv_pk_mul_f32 v[186:187], v[186:187], v[202:203]\nv_pk_mul_f32 v[188:189], v[188:189], v[204:205]\n" \
  "s_waitcnt lgkmcnt(0)\nv_exp_f32 v190, v158\nv_mfma_f32_32x32x16_bf16 v[0:15], v[242:245], v[128:131], v[0:15]\nv_exp_f32 v191, v159\n" \
  "v_exp_f32 v192, v160\nv_exp_f32 v193, v161\nv_mfma_f32_32x32x16_bf16 v[0:15], v[246:249], v[132:135], v[0:15]\nv_exp_f32 v194, v162\n" \
  "v_exp_f32 v195, v163\nv_exp_f32 v196, v164\nv_exp_f32 v197, v165\nv_exp_f32 v198, v166\nv_exp_f32 v199, v167\nv_exp_f32 v200, v168\n" \
  "v_exp_f32 v201, v169\nv_exp_f32 v202, v170\nv_exp_f32 v203, v171\nv_exp_f32 v204, v172\nv_exp_f32 v205, v173\nv_cvt_pk_bf16_f32 v128, v174, v175\n" \
  "v_cvt_pk_bf16_f32 v129, v176, v177\nv_mfma_f32_16x16x4_4b_bf16 v[190:205], v[240:241], v[240:241], v[190:205]\nv_cvt_pk_bf16_f32 v130, v178, v179\n" \
  "v_cvt_pk_bf16_f32 v131, v180, v181\nv_cvt_pk_bf16_f32 v132, v182, v183\nv_cvt_pk_bf16_f32 v133, v184, v185\nv_cvt_pk_bf16_f32 v134, v186, v187\n" \
  "v_cvt_pk_bf16_f32 v135, v188, v189\ns_waitcnt vmcnt(0)\nds_read_b128 v[206:209], v144 offset:0\nds_read_b128 v[210:213], v144 offset:32\n" \
  "ds_read_b128 v[214:217], v145 offset:0\nds_read_b128 v[218:221], v145 offset:32\nds_read_b128 v[222:225], v144 offset:4160\n" \
  "ds_read_b128 v[226:229], v144 offset:4192\nv_mfma_f32_32x32x16_bf16 v[174:189], v[230:233], v[136:139], 0\nv_rcp_f32 v190, v190\n" \
  "v_rcp_f32 v191, v191\nv_rcp_f32 v192, v192\nv_mfma_f32_32x32x16_bf16 v[174:189], v[234:237], v[140:143], v[174:189]\nv_rcp_f32 v193, v193\n" \
  "v_rcp_f32 v194, v194\nv_rcp_f32 v195, v195\nv_mfma_f32_32x32x8_bf16 v[174:189], v[238:239], v[126:127], v[174:189]\nv_rcp_f32 v196, v196\n" \
  "v_rcp_f32 v197, v197\nv_rcp_f32 v198, v198\nv_rcp_f32 v199, v199\nv_rcp_f32 v200, v200\nv_rcp_f32 v201, v201\nv_rcp_f32 v202, v202\n" \
  "v_rcp_f32 v203, v203\nv_rcp_f32 v204, v204\nv_rcp_f32 v205, v205\nv_pk_mul_f32 v[158:159], v[158:159], v[190:191]\n" \
  "v_pk_mul_f32 v[160:161], v[160:161], v[192:193]\nv_pk_mul_f32 v[162:163], v[162:163], v[194:195]\nv_pk_mul_f32 v[164:165], v[164:165], v[196:197]\n" \
  "v_pk_mul_f32 v[166:167], v[166:167], v[198:199]\nv_pk_mul_f32 v[168:169], v[168:169], v[200:201]\nv_pk_mul_f32 v[170:171], v[170:171], v[202:203]\n" \
  "v_pk_mul_f32 v[172:173], v[172:173], v[204:205]\nds_read_b64_tr_b16 v[238:239], v146\nds_read_b128 v[230:233], v145 offset:4160\n" \
  "ds_read_b128 v[234:237], v145 offset:4192\nds_read_b64 v[250:251], v148 offset:64\nv_exp_f32 v190, v174\n" \
  "v_mfma_f32_32x32x16_bf16 v[16:31], v[242:245], v[128:131], v[16:31]\nv_exp_f32 v191, v175\nv_exp_f32 v192, v176\nv_exp_f32 v193, v177\n" \
  "v_mfma_f32_32x32x16_bf16 v[16:31], v[246:249], v[132:135], v[16:31]\nv_exp_f32 v194, v178\nv_exp_f32 v195, v179\nv_exp_f32 v196, v180\n" \
  "v_exp_f32 v197, v181\nv_exp_f32 v198, v182\nv_exp_f32 v199, v183\nv_exp_f32 v200, v184\nv_exp_f32 v201, v185\nv_exp_f32 v202, v186\n" \
  "v_exp_f32 v203, v187\nv_exp_f32 v204, v188\nv_exp_f32 v205, v189\nv_cvt_pk_bf16_f32 v128, v158, v159\nv_cvt_pk_bf16_f32 v129, v160, v161\n" \
  "v_mfma_f32_16x16x4_4b_bf16 v[190:205], v[240:241], v[240:241], v[190:205]\nv_cvt_pk_bf16_f32 v130, v162, v163\nv_cvt_pk_bf16_f32 v131, v164, v165\n" \
  "v_cvt_pk_bf16_f32 v132, v166, v167\nv_cvt_pk_bf16_f32 v133, v168, v169\nv_cvt_pk_bf16_f32 v134, v170, v171\nv_cvt_pk_bf16_f32 v135, v172, v173\n" \
  "s_waitcnt lgkmcnt(0)\ns_mov_b32 m0, %[stg]\nv_perm_b32 v252, v250, v149, s82\nv_perm_b32 v253, v250, v149, s83\nv_perm_b32 v254, v251, v149, s82\n" \
  "v_perm_b32 v255, v251, v149, s83\nbuffer_load_dwordx4 v252, %[rb], s84 offen offset:0 sc1 lds\n" \
  "buffer_load_dwordx4 v253, %[rb], s85 offen offset:1040 sc1 lds\nbuffer_load_dwordx4 v254, %[rb], s86 offen offset:2080 sc1 lds\n" \
  "buffer_load_dwordx4 v255, %[rb], s87 offen offset:3120 sc1 lds\ns_add_u32 m0, %[stg], 4160\ns_nop 0\n" \
  "buffer_load_dwordx4 v252, %[rb], s88 offen offset:0 sc1 lds\nbuffer_load_dwordx4 v253, %[rb], s89 offen offset:1040 sc1 lds\n" \
  "buffer_load_dwordx4 v254, %[rb], s90 offen offset:2080 sc1 lds\nbuffer_load_dwordx4 v255, %[rb], s91 offen offset:3120 sc1 lds\n" \
  "v_mfma_f32_32x32x16_bf16 v[158:173], v[206:209], v[136:139], 0\nv_rcp_f32 v190, v190\nv_rcp_f32 v191, v191\nv_rcp_f32 v192, v192\n" \
  "v_mfma_f32_32x32x16_bf16 v[158:173], v[210:213], v[140:143], v[158:173]\nv_rcp_f32 v193, v193\nv_rcp_f32 v194, v194\nv_rcp_f32 v195, v195\n" \
  "v_mfma_f32_32x32x8_bf16 v[158:173], v[238:239], v[120:121], v[158:173]\nv_rcp_f32 v196, v196\nv_rcp_f32 v197, v197\nv_rcp_f32 v198, v198\n" \
  "v_rcp_f32 v199, v199\nv_rcp_f32 v200, v200\nv_rcp_f32 v201, v201\nv_rcp_f32 v202, v202\nv_rcp_f32 v203, v203\nv_rcp_f32 v204, v204\n" \
  "v_rcp_f32 v205, v205\nv_pk_mul_f32 v[174:175], v[174:175], v[190:191]\nv_pk_mul_f32 v[176:177], v[176:177], v[192:193]\n" \
  "v_pk_mul_f32 v[178:179], v[178:179], v[194:195]\nv_pk_mul_f32 v[180:181], v[180:181], v[196:197]\nv_pk_mul_f32 v[182:183], v[182:183], v[198:199]\n" \
  "v_pk_mul_f32 v[184:185], v[184:185], v[200:201]\nv_pk_mul_f32 v[186:187], v[186:187], v[202:203]\nv_pk_mul_f32 v[188:189], v[188:189], v[204:205]\n" \
  "s_cmp_gt_i32 %[rem], 56\ns_cbranch_scc1 .Lloop%=\ns_cmp_gt_i32 %[rem], 24\ns_cbranch_scc1 .Ltop%=\n" \
  "v_mfma_f32_32x32x16_bf16 v[32:47], v[242:245], v[128:131], v[32:47]\nv_mfma_f32_32x32x16_bf16 v[32:47], v[246:249], v[132:135], v[32:47]\n" \
  "v_cvt_pk_bf16_f32 v128, v174, v175\nv_cvt_pk_bf16_f32 v129, v176, v177\nv_cvt_pk_bf16_f32 v130, v178, v179\nv_cvt_pk_bf16_f32 v131, v180, v181\n" \
  "v_cvt_pk_bf16_f32 v132, v182, v183\nv_cvt_pk_bf16_f32 v133, v184, v185\nv_cvt_pk_bf16_f32 v134, v186, v187\nv_cvt_pk_bf16_f32 v135, v188, v189\n" \
  ".Ltail%=:\ns_setprio 0\ns_cmp_lt_i32 %[rem], 1\ns_cbranch_scc1 .Lfin2%=\ns_cmp_lt_i32 %[rem], 9\ns_cbranch_scc1 .Lt4%=\ns_cmp_lt_i32 %[rem], 17\n" \
  "s_cbranch_scc1 .Lt8%=\nv_exp_f32 v190, v158\nv_exp_f32 v191, v159\nv_mfma_f32_32x32x16_bf16 v[48:63], v[242:245], v[128:131], v[48:63]\n" \
  "v_exp_f32 v192, v160\nv_exp_f32 v193, v161\nv_exp_f32 v194, v162\nv_mfma_f32_32x32x16_bf16 v[48:63], v[246:249], v[132:135], v[48:63]\n" \
  "v_exp_f32 v195, v163\nv_exp_f32 v196, v164\nds_read_b64 v[242:243], v147 offset:0\nv_exp_f32 v197, v165\nds_read_b64 v[244:245], v147 offset:16\n" \
  "v_exp_f32 v198, v166\nds_read_b64 v[246:247], v147 offset:32\nv_exp_f32 v199, v167\nds_read_b64 v[248:249], v147 offset:48\nv_exp_f32 v200, v168\n" \
  "v_exp_f32 v201, v169\nv_mov_b32 v134, 0\nv_mov_b32 v135, 0\nv_pk_add_f32 v[190:191], v[190:191], 1.0 op_sel_hi:[1,0]\n" \
  "v_pk_add_f32 v[192:193], v[192:193], 1.0 op_sel_hi:[1,0]\nv_add_u32 v147, 64, v147\nv_pk_add_f32 v[194:195], v[194:195], 1.0 op_sel_hi:[1,0]\n" \
  "v_pk_add_f32 v[196:197], v[196:197], 1.0 op_sel_hi:[1,0]\nv_pk_add_f32 v[198:199], v[198:199], 1.0 op_sel_hi:[1,0]\n" \
  "v_pk_add_f32 v[200:201], v[200:201], 1.0 op_sel_hi:[1,0]\nv_mfma_f32_32x32x16_bf16 v[174:189], v[214:217], v[136:139], 0\nv_rcp_f32 v190, v190\n" \
  "v_rcp_f32 v191, v191\nv_rcp_f32 v192, v192\nv_mfma_f32_32x32x16_bf16 v[174:189], v[218:221], v[140:143], v[174:189]\nv_rcp_f32 v193, v193\n" \
  "v_rcp_f32 v194, v194\nv_rcp_f32 v195, v195\nv_mfma_f32_32x32x8_bf16 v[174:189], v[238:239], v[122:123], v[174:189]\nv_rcp_f32 v196, v196\n" \
  "v_rcp_f32 v197, v197\nv_rcp_f32 v198, v198\nv_rcp_f32 v199, v199\nv_rcp_f32 v200, v200\nv_rcp_f32 v201, v201\n" \
  "v_pk_mul_f32 v[158:159], v[158:159], v[190:191]\nv_pk_mul_f32 v[160:161], v[160:161], v[192:193]\nv_pk_mul_f32 v[162:163], v[162:163], v[194:195]\n" \
  "v_pk_mul_f32 v[164:165], v[164:165], v[196:197]\nv_pk_mul_f32 v[166:167], v[166:167], v[198:199]\nv_pk_mul_f32 v[168:169], v[168:169], v[200:201]\n" \
  "v_cvt_pk_bf16_f32 v128, v158, v159\nv_cvt_pk_bf16_f32 v129, v160, v161\nv_cvt_pk_bf16_f32 v130, v162, v163\nv_cvt_pk_bf16_f32 v131, v164, v165\n" \
  "v_cvt_pk_bf16_f32 v132, v166, v167\nv_cvt_pk_bf16_f32 v133, v168, v169\ns_waitcnt lgkmcnt(0)\nv_exp_f32 v190, v174\nv_exp_f32 v191, v175\n" \
  "v_mfma_f32_32x32x16_bf16 v[0:15], v[242:245], v[128:131], v[0:15]\nv_exp_f32 v192, v176\nv_exp_f32 v193, v177\nv_exp_f32 v194, v178\n" \
  "v_mfma_f32_32x32x16_bf16 v[0:15], v[246:249], v[132:135], v[0:15]\nv_exp_f32 v195, v179\nv_exp_f32 v196, v180\nv_exp_f32 v197, v181\n" \
  "v_exp_f32 v198, v182\nv_exp_f32 v199, v183\nv_exp_f32 v200, v184\nv_exp_f32 v201, v185\nv_pk_add_f32 v[190:191], v[190:191], 1.0 op_sel_hi:[1,0]\n" \
  "v_pk_add_f32 v[192:193], v[192:193], 1.0 op_sel_hi:[1,0]\nv_pk_add_f32 v[194:195], v[194:195], 1.0 op_sel_hi:[1,0]\n" \
  "v_pk_add_f32 v[196:197], v[196:197], 1.0 op_sel_hi:[1,0]\nv_pk_add_f32 v[198:199], v[198:199], 1.0 op_sel_hi:[1,0]\n" \
  "v_pk_add_f32 v[200:201], v[200:201], 1.0 op_sel_hi:[1,0]\nv_mfma_f32_32x32x16_bf16 v[158:173], v[222:225], v[136:139], 0\nv_rcp_f32 v190, v190\n" \
  "v_rcp_f32 v191, v191\nv_rcp_f32 v192, v192\nv_mfma_f32_32x32x16_bf16 v[158:173], v[226:229], v[140:143], v[158:173]\nv_rcp_f32 v193, v193\n" \
  "v_rcp_f32 v194, v194\nv_rcp_f32 v195, v195\nv_mfma_f32_32x32x8_bf16 v[158:173], v[238:239], v[124:125], v[158:173]\nv_rcp_f32 v196, v196\n" \
  "v_rcp_f32 v197, v197\nv_rcp_f32 v198, v198\nv_rcp_f32 v199, v199\nv_rcp_f32 v200, v200\nv_rcp_f32 v201, v201\n" \
  "v_pk_mul_f32 v[174:175], v[174:175], v[190:191]\nv_pk_mul_f32 v[176:177], v[176:177], v[192:193]\nv_pk_mul_f32 v[178:179], v[178:179], v[194:195]\n" \
  "v_pk_mul_f32 v[180:181], v[180:181], v[196:197]\nv_pk_mul_f32 v[182:183], v[182:183], v[198:199]\nv_pk_mul_f32 v[184:185], v[184:185], v[200:201]\n" \
  "v_cvt_pk_bf16_f32 v128, v174, v175\nv_cvt_pk_bf16_f32 v129, v176, v177\nv_cvt_pk_bf16_f32 v130, v178, v179\nv_cvt_pk_bf16_f32 v131, v180, v181\n" \
  "v_cvt_pk_bf16_f32 v132, v182, v183\nv_cvt_pk_bf16_f32 v133, v184, v185\nv_exp_f32 v190, v158\nv_exp_f32 v191, v159\n" \
  "v_mfma_f32_32x32x16_bf16 v[16:31], v[242:245], v[128:131], v[16:31]\nv_exp_f32 v192, v160\nv_exp_f32 v193, v161\nv_exp_f32 v194, v162\n" \
  "v_mfma_f32_32x32x16_bf16 v[16:31], v[246:249], v[132:135], v[16:31]\nv_exp_f32 v195, v163\nv_exp_f32 v196, v164\nv_exp_f32 v197, v165\n" \
  "v_exp_f32 v198, v166\nv_exp_f32 v199, v167\nv_exp_f32 v200, v168\nv_exp_f32 v201, v169\nv_pk_add_f32 v[190:191], v[190:191], 1.0 op_sel_hi:[1,0]\n" \
  "v_pk_add_f32 v[192:193], v[192:193], 1.0 op_sel_hi:[1,0]\nv_pk_add_f32 v[194:195], v[194:195], 1.0 op_sel_hi:[1,0]\n" \
  "v_pk_add_f32 v[196:197], v[196:197], 1.0 op_sel_hi:[1,0]\nv_pk_add_f32 v[198:199], v[198:199], 1.0 op_sel_hi:[1,0]\n" \
  "v_pk_add_f32 v[200:201], v[200:201], 1.0 op_sel_hi:[1,0]\nv_mfma_f32_32x32x16_bf16 v[174:189], v[230:233], v[136:139], 0\nv_rcp_f32 v190, v190\n" \
  "v_rcp_f32 v191, v191\nv_rcp_f32 v192, v192\nv_mfma_f32_32x32x16_bf16 v[174:189], v[234:237], v[140:143], v[174:189]\nv_rcp_f32 v193, v193\n" \
  "v_rcp_f32 v194, v194\nv_rcp_f32 v195, v195\nv_mfma_f32_32x32x8_bf16 v[174:189], v[238:239], v[126:127], v[174:189]\nv_rcp_f32 v196, v196\n" \
  "v_rcp_f32 v197, v197\nv_rcp_f32 v198, v198\nv_rcp_f32 v199, v199\nv_rcp_f32 v200, v200\nv_rcp_f32 v201, v201\n" \
  "v_pk_mul_f32 v[158:159], v[158:159], v[190:191]\nv_pk_mul_f32 v[160:161], v[160:161], v[192:193]\nv_pk_mul_f32 v[162:163], v[162:163], v[194:195]\n" \
  "v_pk_mul_f32 v[164:165], v[164:165], v[196:197]\nv_pk_mul_f32 v[166:167], v[166:167], v[198:199]\nv_pk_mul_f32 v[168:169], v[168:169], v[200:201]\n" \
  "v_cvt_pk_bf16_f32 v128, v158, v159\nv_cvt_pk_bf16_f32 v129, v160, v161\nv_cvt_pk_bf16_f32 v130, v162, v163\nv_cvt_pk_bf16_f32 v131, v164, v165\n" \
  "v_cvt_pk_bf16_f32 v132, v166, v167\nv_cvt_pk_bf16_f32 v133, v168, v169\nv_exp_f32 v190, v174\nv_exp_f32 v191, v175\n" \
  "v_mfma_f32_32x32x16_bf16 v[32:47], v[242:245], v[128:131], v[32:47]\nv_exp_f32 v192, v176\nv_exp_f32 v193, v177\nv_exp_f32 v194, v178\n" \
  "v_mfma_f32_32x32x16_bf16 v[32:47], v[246:249], v[132:135], v[32:47]\nv_exp_f32 v195, v179\nv_exp_f32 v196, v180\nv_exp_f32 v197, v181\n" \
  "v_exp_f32 v198, v182\nv_exp_f32 v199, v183\nv_exp_f32 v200, v184\nv_exp_f32 v201, v185\nv_pk_add_f32 v[190:191], v[190:191], 1.0 op_sel_hi:[1,0]\n" \
  "v_pk_add_f32 v[192:193], v[192:193], 1.0 op_sel_hi:[1,0]\nv_pk_add_f32 v[194:195], v[194:195], 1.0 op_sel_hi:[1,0]\n" \
  "v_pk_add_f32 v[196:197], v[196:197], 1.0 op_sel_hi:[1,0]\nv_pk_add_f32 v[198:199], v[198:199], 1.0 op_sel_hi:[1,0]\n" \
  "v_pk_add_f32 v[200:201], v[200:201], 1.0 op_sel_hi:[1,0]\nv_rcp_f32 v190, v190\nv_rcp_f32 v191, v191\nv_rcp_f32 v192, v192\nv_rcp_f32 v193, v193\n" \
  "v_rcp_f32 v194, v194\nv_rcp_f32 v195, v195\nv_rcp_f32 v196, v196\nv_rcp_f32 v197, v197\nv_rcp_f32 v198, v198\nv_rcp_f32 v199, v199\n" \
  "v_rcp_f32 v200, v200\nv_rcp_f32 v201, v201\nv_pk_mul_f32 v[174:175], v[174:175], v[190:191]\nv_pk_mul_f32 v[176:177], v[176:177], v[192:193]\n" \
  "v_pk_mul_f32 v[178:179], v[178:179], v[194:195]\nv_pk_mul_f32 v[180:181], v[180:181], v[196:197]\nv_pk_mul_f32 v[182:183], v[182:183], v[198:199]\n" \
  "v_pk_mul_f32 v[184:185], v[184:185], v[200:201]\nv_cvt_pk_bf16_f32 v128, v174, v175\nv_cvt_pk_bf16_f32 v129, v176, v177\n" \
  "v_cvt_pk_bf16_f32 v130, v178, v179\nv_cvt_pk_bf16_f32 v131, v180, v181\nv_cvt_pk_bf16_f32 v132, v182, v183\nv_cvt_pk_bf16_f32 v133, v184, v185\n" \
  ".Lfin2%=:\ns_nop 1\nv_mfma_f32_32x32x16_bf16 v[48:63], v[246:249], v[132:135], v[48:63]\ns_branch .Lfin1%=\n.Lt8%=:\n" \
  "v_mfma_f32_32x32x16_bf16 v[174:189], v[214:217], v[136:139], 0\nv_exp_f32 v190, v158\nv_exp_f32 v191, v159\nv_exp_f32 v192, v160\n" \
  "v_mfma_f32_32x32x16_bf16 v[48:63], v[242:245], v[128:131], v[48:63]\nv_exp_f32 v193, v161\nv_exp_f32 v194, v162\nv_exp_f32 v195, v163\n" \
  "v_exp_f32 v196, v164\nv_exp_f32 v197, v165\nv_pk_add_f32 v[190:191], v[190:191], 1.0 op_sel_hi:[1,0]\n" \
  "v_pk_add_f32 v[192:193], v[192:193], 1.0 op_sel_hi:[1,0]\nv_pk_add_f32 v[194:195], v[194:195], 1.0 op_sel_hi:[1,0]\n" \
  "v_pk_add_f32 v[196:197], v[196:197], 1.0 op_sel_hi:[1,0]\nv_mfma_f32_32x32x16_bf16 v[48:63], v[246:249], v[132:135], v[48:63]\nv_rcp_f32 v190, v190\n" \
  "ds_read_b64 v[242:243], v147 offset:0\nv_rcp_f32 v191, v191\nds_read_b64 v[244:245], v147 offset:16\nv_rcp_f32 v192, v192\n" \
  "v_mfma_f32_32x32x16_bf16 v[174:189], v[218:221], v[140:143], v[174:189]\nds_read_b64 v[246:247], v147 offset:32\nv_rcp_f32 v193, v193\n" \
  "ds_read_b64 v[248:249], v147 offset:48\nv_rcp_f32 v194, v194\nv_rcp_f32 v195, v195\n" \
  "v_mfma_f32_32x32x8_bf16 v[174:189], v[238:239], v[122:123], v[174:189]\nv_rcp_f32 v196, v196\nv_rcp_f32 v197, v197\n" \
  "v_pk_mul_f32 v[158:159], v[158:159], v[190:191]\nv_pk_mul_f32 v[160:161], v[160:161], v[192:193]\nv_pk_mul_f32 v[162:163], v[162:163], v[194:195]\n" \
  "v_pk_mul_f32 v[164:165], v[164:165], v[196:197]\nv_cvt_pk_bf16_f32 v128, v158, v159\nv_cvt_pk_bf16_f32 v129, v160, v161\n" \
  "v_cvt_pk_bf16_f32 v130, v162, v163\nv_cvt_pk_bf16_f32 v131, v164, v165\ns_nop 3\ns_waitcnt lgkmcnt(0)\n" \
  "v_mfma_f32_32x32x16_bf16 v[158:173], v[222:225], v[136:139], 0\nv_exp_f32 v190, v174\nv_exp_f32 v191, v175\nv_exp_f32 v192, v176\n" \
  "v_mfma_f32_32x32x16_bf16 v[0:15], v[242:245], v[128:131], v[0:15]\nv_exp_f32 v193, v177\nv_exp_f32 v194, v178\nv_exp_f32 v195, v179\n" \
  "v_exp_f32 v196, v180\nv_exp_f32 v197, v181\nv_pk_add_f32 v[190:191], v[190:191], 1.0 op_sel_hi:[1,0]\n" \
  "v_pk_add_f32 v[192:193], v[192:193], 1.0 op_sel_hi:[1,0]\nv_pk_add_f32 v[194:195], v[194:195], 1.0 op_sel_hi:[1,0]\n" \
  "v_pk_add_f32 v[196:197], v[196:197], 1.0 op_sel_hi:[1,0]\nv_mfma_f32_32x32x16_bf16 v[158:173], v[226:229], v[140:143], v[158:173]\n" \
  "v_rcp_f32 v190, v190\nv_rcp_f32 v191, v191\nv_rcp_f32 v192, v192\nv_mfma_f32_32x32x8_bf16 v[158:173], v[238:239], v[124:125], v[158:173]\n" \
  "v_rcp_f32 v193, v193\nv_rcp_f32 v194, v194\nv_rcp_f32 v195, v195\nv_rcp_f32 v196, v196\nv_rcp_f32 v197, v197\n" \
  "v_pk_mul_f32 v[174:175], v[174:175], v[190:191]\nv_pk_mul_f32 v[176:177], v[176:177], v[192:193]\nv_pk_mul_f32 v[178:179], v[178:179], v[194:195]\n" \
  "v_pk_mul_f32 v[180:181], v[180:181], v[196:197]\nv_cvt_pk_bf16_f32 v128, v174, v175\nv_cvt_pk_bf16_f32 v129, v176, v177\n" \
  "v_cvt_pk_bf16_f32 v130, v178, v179\nv_cvt_pk_bf16_f32 v131, v180, v181\nv_mfma_f32_32x32x16_bf16 v[174:189], v[230:233], v[136:139], 0\n" \
  "v_exp_f32 v190, v158\nv_exp_f32 v191, v159\nv_exp_f32 v192, v160\nv_mfma_f32_32x32x16_bf16 v[16:31], v[242:245], v[128:131], v[16:31]\n" \
  "v_exp_f32 v193, v161\nv_exp_f32 v194, v162\nv_exp_f32 v195, v163\nv_exp_f32 v196, v164\nv_exp_f32 v197, v165\n" \
  "v_pk_add_f32 v[190:191], v[190:191], 1.0 op_sel_hi:[1,0]\nv_pk_add_f32 v[192:193], v[192:193], 1.0 op_sel_hi:[1,0]\n" \
  "v_pk_add_f32 v[194:195], v[194:195], 1.0 op_sel_hi:[1,0]\nv_pk_add_f32 v[196:197], v[196:197], 1.0 op_sel_hi:[1,0]\n" \
  "v_mfma_f32_32x32x16_bf16 v[174:189], v[234:237], v[140:143], v[174:189]\nv_rcp_f32 v190, v190\nv_rcp_f32 v191, v191\nv_rcp_f32 v192, v192\n" \
  "v_mfma_f32_32x32x8_bf16 v[174:189], v[238:239], v[126:127], v[174:189]\nv_rcp_f32 v193, v193\nv_rcp_f32 v194, v194\nv_rcp_f32 v195, v195\n" \
  "v_rcp_f32 v196, v196\nv_rcp_f32 v197, v197\nv_pk_mul_f32 v[158:159], v[158:159], v[190:191]\nv_pk_mul_f32 v[160:161], v[160:161], v[192:193]\n" \
  "v_pk_mul_f32 v[162:163], v[162:163], v[194:195]\nv_pk_mul_f32 v[164:165], v[164:165], v[196:197]\nv_cvt_pk_bf16_f32 v128, v158, v159\n" \
  "v_cvt_pk_bf16_f32 v129, v160, v161\nv_cvt_pk_bf16_f32 v130, v162, v163\nv_cvt_pk_bf16_f32 v131, v164, v165\nv_exp_f32 v190, v174\n" \
  "v_exp_f32 v191, v175\nv_mfma_f32_32x32x16_bf16 v[32:47], v[242:245], v[128:131], v[32:47]\nv_exp_f32 v192, v176\nv_exp_f32 v193, v177\n" \
  "v_exp_f32 v194, v178\nv_exp_f32 v195, v179\nv_exp_f32 v196, v180\nv_exp_f32 v197, v181\nv_pk_add_f32 v[190:191], v[190:191], 1.0 op_sel_hi:[1,0]\n" \
  "v_pk_add_f32 v[192:193], v[192:193], 1.0 op_sel_hi:[1,0]\nv_pk_add_f32 v[194:195], v[194:195], 1.0 op_sel_hi:[1,0]\n" \
  "v_pk_add_f32 v[196:197], v[196:197], 1.0 op_sel_hi:[1,0]\nv_rcp_f32 v190, v190\nv_rcp_f32 v191, v191\nv_rcp_f32 v192, v192\nv_rcp_f32 v193, v193\n" \
  "v_rcp_f32 v194, v194\nv_rcp_f32 v195, v195\nv_rcp_f32 v196, v196\nv_rcp_f32 v197, v197\nv_pk_mul_f32 v[174:175], v[174:175], v[190:191]\n" \
  "v_pk_mul_f32 v[176:177], v[176:177], v[192:193]\nv_pk_mul_f32 v[178:179], v[178:179], v[194:195]\nv_pk_mul_f32 v[180:181], v[180:181], v[196:197]\n" \
  "v_cvt_pk_bf16_f32 v128, v174, v175\nv_cvt_pk_bf16_f32 v129, v176, v177\nv_cvt_pk_bf16_f32 v130, v178, v179\nv_cvt_pk_bf16_f32 v131, v180, v181\n" \
  "s_branch .Lfin1%=\n.Lt4%=:\nv_mfma_f32_32x32x16_bf16 v[174:189], v[214:217], v[136:139], 0\nv_exp_f32 v190, v158\nv_exp_f32 v191, v159\n" \
  "v_exp_f32 v192, v160\nv_mfma_f32_32x32x16_bf16 v[48:63], v[242:245], v[128:131], v[48:63]\nv_exp_f32 v193, v161\nv_add_f32 v190, 1.0, v190\n" \
  "v_add_f32 v191, 1.0, v191\nv_mfma_f32_32x32x16_bf16 v[48:63], v[246:249], v[132:135], v[48:63]\nv_add_f32 v192, 1.0, v192\nv_add_f32 v193, 1.0, v193\n" \
  "v_mfma_f32_32x32x16_bf16 v[174:189], v[218:221], v[140:143], v[174:189]\nds_read_b64 v[242:243], v147 offset:0\nv_rcp_f32 v190, v190\n" \
  "ds_read_b64 v[244:245], v147 offset:16\nv_rcp_f32 v191, v191\nds_read_b64 v[246:247], v147 offset:32\nv_rcp_f32 v192, v192\n" \
  "v_mfma_f32_32x32x8_bf16 v[174:189], v[238:239], v[122:123], v[174:189]\nds_read_b64 v[248:249], v147 offset:48\nv_rcp_f32 v193, v193\n" \
  "v_mul_f32 v158, v158, v190\nv_mul_f32 v159, v159, v191\nv_mul_f32 v160, v160, v192\nv_mul_f32 v161, v161, v193\nv_cvt_pk_bf16_f32 v128, v158, v159\n" \
  "v_cvt_pk_bf16_f32 v129, v160, v161\nv_mov_b32 v130, 0\nv_mov_b32 v131, 0\ns_nop 4\ns_waitcnt lgkmcnt(0)\n" \
  "v_mfma_f32_32x32x16_bf16 v[158:173], v[222:225], v[136:139], 0\nv_exp_f32 v190, v174\nv_exp_f32 v191, v175\nv_exp_f32 v192, v176\n" \
  "v_mfma_f32_32x32x16_bf16 v[0:15], v[242:245], v[128:131], v[0:15]\nv_exp_f32 v193, v177\nv_add_f32 v190, 1.0, v190\nv_add_f32 v191, 1.0, v191\n" \
  "v_add_f32 v192, 1.0, v192\nv_add_f32 v193, 1.0, v193\nv_mfma_f32_32x32x16_bf16 v[158:173], v[226:229], v[140:143], v[158:173]\nv_rcp_f32 v190, v190\n" \
  "v_rcp_f32 v191, v191\nv_rcp_f32 v192, v192\nv_mfma_f32_32x32x8_bf16 v[158:173], v[238:239], v[124:125], v[158:173]\nv_rcp_f32 v193, v193\n" \
  "v_mul_f32 v174, v174, v190\nv_mul_f32 v175, v175, v191\nv_mul_f32 v176, v176, v192\nv_mul_f32 v177, v177, v193\nv_cvt_pk_bf16_f32 v128, v174, v175\n" \
  "v_cvt_pk_bf16_f32 v129, v176, v177\ns_nop 4\nv_mfma_f32_32x32x16_bf16 v[174:189], v[230:233], v[136:139], 0\nv_exp_f32 v190, v158\n" \
  "v_exp_f32 v191, v159\nv_exp_f32 v192, v160\nv_mfma_f32_32x32x16_bf16 v[16:31], v[242:245], v[128:131], v[16:31]\nv_exp_f32 v193, v161\n" \
  "v_add_f32 v190, 1.0, v190\nv_add_f32 v191, 1.0, v191\nv_add_f32 v192, 1.0, v192\nv_add_f32 v193, 1.0, v193\n" \
  "v_mfma_f32_32x32x16_bf16 v[174:189], v[234:237], v[140:143], v[174:189]\nv_rcp_f32 v190, v190\nv_rcp_f32 v191, v191\nv_rcp_f32 v192, v192\n" \
  "v_mfma_f32_32x32x8_bf16 v[174:189], v[238:239], v[126:127], v[174:189]\nv_rcp_f32 v193, v193\nv_mul_f32 v158, v158, v190\nv_mul_f32 v159, v159, v191\n" \
  "v_mul_f32 v160, v160, v192\nv_mul_f32 v161, v161, v193\nv_cvt_pk_bf16_f32 v128, v158, v159\nv_cvt_pk_bf16_f32 v129, v160, v161\ns_nop 4\n" \
  "v_exp_f32 v190, v174\nv_exp_f32 v191, v175\nv_mfma_f32_32x32x16_bf16 v[32:47], v[242:245], v[128:131], v[32:47]\nv_exp_f32 v192, v176\n" \
  "v_exp_f32 v193, v177\nv_add_f32 v190, 1.0, v190\nv_add_f32 v191, 1.0, v191\nv_add_f32 v192, 1.0, v192\nv_add_f32 v193, 1.0, v193\n" \
  "v_rcp_f32 v190, v190\nv_rcp_f32 v191, v191\nv_rcp_f32 v192, v192\nv_rcp_f32 v193, v193\nv_mul_f32 v174, v174, v190\nv_mul_f32 v175, v175, v191\n" \
  "v_mul_f32 v176, v176, v192\nv_mul_f32 v177, v177, v193\nv_cvt_pk_bf16_f32 v128, v174, v175\nv_cvt_pk_bf16_f32 v129, v176, v177\n.Lfin1%=:\ns_nop 1\n" \
  "v_mfma_f32_32x32x16_bf16 v[48:63], v[242:245], v[128:131], v[48:63]\ns_cmp_lg_u32 s68, 0\ns_cbranch_scc1 .Lp2%=\ns_mul_i32 s94, %[win], 144\n" \
  "s_and_b32 s94, s94, 255\ns_add_u32 s94, s94, s95\nv_add_u32 v158, s94, v78\nv_add_u32 v159, s95, v79\nv_cvt_pk_bf16_f32 v170, v0, v16\n" \
  "v_cvt_pk_bf16_f32 v172, v4, v20\nv_cvt_pk_bf16_f32 v174, v8, v24\nds_write_b16 v158, v170 offset:0\nds_write_b16_d16_hi v158, v170 offset:16\n" \
  "ds_write_b16 v158, v172 offset:160\nds_write_b16_d16_hi v158, v172 offset:176\nds_write_b16 v158, v174 offset:320\n" \
  "ds_write_b16_d16_hi v158, v174 offset:336\nv_mul_f32 v160, v1, v80\nv_mul_f32 v161, v2, v80\nv_mul_f32 v162, v3, v80\nv_mul_f32 v163, v5, v80\n" \
  "v_mul_f32 v164, v6, v80\nv_mul_f32 v165, v7, v80\nv_mul_f32 v166, v9, v80\nv_mul_f32 v167, v10, v80\nv_mul_f32 v168, v11, v80\n" \
  "v_cvt_pk_bf16_f32 v171, v32, v48\nv_cvt_pk_bf16_f32 v173, v36, v52\nv_cvt_pk_bf16_f32 v175, v40, v56\nds_write_b16 v158, v171 offset:32\n" \
  "ds_write_b16_d16_hi v158, v171 offset:48\nds_write_b16 v158, v173 offset:192\nds_write_b16_d16_hi v158, v173 offset:208\n" \
  "ds_write_b16 v158, v175 offset:352\nds_write_b16_d16_hi v158, v175 offset:368\nv_fmac_f32 v160, v17, v81\nv_fmac_f32 v161, v18, v81\n" \
  "v_fmac_f32 v162, v19, v81\nv_fmac_f32 v163, v21, v81\nv_fmac_f32 v164, v22, v81\nv_fmac_f32 v165, v23, v81\nv_fmac_f32 v166, v25, v81\n" \
  "v_fmac_f32 v167, v26, v81\nv_fmac_f32 v168, v27, v81\nv_fmac_f32 v160, v33, v82\nv_fmac_f32 v161, v34, v82\nv_fmac_f32 v162, v35, v82\n" \
  "v_fmac_f32 v163, v37, v82\nv_fmac_f32 v164, v38, v82\nv_fmac_f32 v165, v39, v82\nv_fmac_f32 v166, v41, v82\nv_fmac_f32 v167, v42, v82\n" \
  "v_fmac_f32 v168, v43, v82\nv_fmac_f32 v160, v49, v83\nv_fmac_f32 v161, v50, v83\nv_fmac_f32 v162, v51, v83\nv_fmac_f32 v163, v53, v83\n" \
  "v_fmac_f32 v164, v54, v83\nv_fmac_f32 v165, v55, v83\nv_fmac_f32 v166, v57, v83\nv_fmac_f32 v167, v58, v83\nv_fmac_f32 v168, v59, v83\n" \
  "ds_write2st64_b32 v159, v160, v161 offset0:9 offset1:10\nds_write2st64_b32 v159, v162, v163 offset0:11 offset1:12\n" \
  "ds_write2st64_b32 v159, v164, v165 offset0:13 offset1:14\nds_write2st64_b32 v159, v166, v167 offset0:15 offset1:16\n" \
  "ds_write_b32 v159, v168 offset:4352\n.Lend%=:\n"
#define CONS_CLOB "v0", "v1", "v2", "v3", "v4", "v5", "v6", "v7", "v8", "v9", "v10", "v11", "v12", "v13", "v14", "v15", "v16", "v17", "v18", "v19", "v20", "v21", "v22", "v23", "v24", "v25", "v26", "v27", "v28", "v29", "v30", "v31", "v32", "v33", "v34", "v35", "v36", "v37", "v38", "v39", "v40", "v41", "v42", "v43", "v44", "v45", "v46", "v47", "v48", "v49", "v50", "v51", "v52", "v53", "v54", "v55", "v56", "v57", "v58", "v59", "v60", "v61", "v62", "v63", "v84", "v85", "v86", "v87", "v120", "v121", "v122", "v123", "v124", "v125", "v126", "v127", "v128", "v129", "v130", "v131", "v132", "v133", "v134", "v135", "v152", "v153", "v154", "v155", "v156", "v158", "v159", "v160", "v161", "v162", "v163", "v164", "v165", "v198", "v199", "v200", "v201", "v202", "v203", "v204", "v205", "v206", "v207", "v208", "v209", "v210", "v211", "v212", "v213", "v214", "v215", "v216", "v217", "v234", "v235", "v236", "v237", "v238", "v239", "v240", "v241", "v242", "v243", "v244", "v245", "v246", "v247", "v248", "v249", "v250", "v251", "v252", "v253", "v254", "v255", "s68", "s69", "s70", "s71", "s72", "s73", "s74", "s75", "s76", "s77", "s78", "s79", "s80", "s81", "s82", "s83", "s84", "s85", "s86", "s87", "s88", "s89", "s90", "s91", "s92", "s93", "s94", "s95", "s96", "s97", "s98", "s99", "vcc", "scc", "memory"
#define EPI_ASM \
  "s_mul_i32 s70, %[win], 10496\ns_mul_i32 s71, %[win], 80\ns_mul_i32 s72, %[win], 30\nv_add_u32 v73, s70, v73\nv_add_u32 v74, s71, v74\n" \
  "ds_read_b128 v[0:3], v64\nds_read_b128 v[16:19], v65\nds_read_b128 v[32:35], v66\nv_xor_b32 v168, 64, v64\nds_read_b128 v[4:7], v168\n" \
  "v_xor_b32 v169, 64, v65\nds_read_b128 v[20:23], v169\nv_xor_b32 v170, 64, v66\nds_read_b128 v[36:39], v170\nv_xor_b32 v171, 128, v64\n" \
  "ds_read_b128 v[8:11], v171\nv_xor_b32 v172, 128, v65\nds_read_b128 v[24:27], v172\nv_xor_b32 v173, 128, v66\nds_read_b128 v[40:43], v173\n" \
  "v_xor_b32 v174, 192, v64\nds_read_b128 v[12:15], v174\nv_xor_b32 v175, 192, v65\nds_read_b128 v[28:31], v175\nv_xor_b32 v176, 192, v66\n" \
  "ds_read_b128 v[44:47], v176\nds_read_b128 v[184:187], v73\nds_read_b128 v[188:191], v73 offset:16\nds_read_b32 v192, v74\ns_waitcnt lgkmcnt(12)\n" \
  "v_mfma_f32_16x16x32_bf16 v[196:199], v[104:107], v[0:3], 0\nv_mfma_f32_16x16x32_bf16 v[200:203], v[104:107], v[16:19], 0\n" \
  "v_mfma_f32_16x16x32_bf16 v[204:207], v[104:107], v[32:35], 0\ns_waitcnt lgkmcnt(9)\n" \
  "v_mfma_f32_16x16x32_bf16 v[196:199], v[108:111], v[4:7], v[196:199]\nv_mfma_f32_16x16x32_bf16 v[200:203], v[108:111], v[20:23], v[200:203]\n" \
  "v_mfma_f32_16x16x32_bf16 v[204:207], v[108:111], v[36:39], v[204:207]\ns_waitcnt lgkmcnt(6)\n" \
  "v_mfma_f32_16x16x32_bf16 v[196:199], v[112:115], v[8:11], v[196:199]\nv_mfma_f32_16x16x32_bf16 v[200:203], v[112:115], v[24:27], v[200:203]\n" \
  "v_mfma_f32_16x16x32_bf16 v[204:207], v[112:115], v[40:43], v[204:207]\ns_waitcnt lgkmcnt(3)\n" \
  "v_mfma_f32_16x16x32_bf16 v[196:199], v[116:119], v[12:15], v[196:199]\nv_mfma_f32_16x16x32_bf16 v[200:203], v[116:119], v[28:31], v[200:203]\n" \
  "v_mfma_f32_16x16x32_bf16 v[204:207], v[116:119], v[44:47], v[204:207]\nds_read_b64 v[178:179], v67\nds_read_b64 v[180:181], v68\n" \
  "ds_read_b64 v[182:183], v69\ns_mov_b32 s73, 0xbfb8aa3b\ns_waitcnt lgkmcnt(3)\nv_add_f32 v184, v184, v188\nv_add_f32 v185, v185, v189\n" \
  "v_add_f32 v186, v186, v190\nv_add_f32 v187, v187, v191\nv_add_f32 v184, v184, v185\nv_add_f32 v186, v186, v187\nv_add_f32 v184, v184, v186\n" \
  "v_mul_f32 v196, s73, v196\nv_mul_f32 v197, s73, v197\nv_add_f32_dpp v184, v184, v184 quad_perm:[1,0,3,2] row_mask:0xf bank_mask:0xf\n" \
  "v_mul_f32 v198, s73, v198\nv_mul_f32 v199, s73, v199\nv_add_f32_dpp v184, v184, v184 quad_perm:[2,3,0,1] row_mask:0xf bank_mask:0xf\n" \
  "s_waitcnt lgkmcnt(0)\ns_barrier\nds_read_b128 v[0:3], v70 offset:0\nds_read_b128 v[4:7], v70 offset:16\nds_read_b128 v[8:11], v70 offset:32\n" \
  "ds_read_b128 v[12:15], v70 offset:48\nds_read_b128 v[16:19], v71 offset:0\nds_read_b128 v[20:23], v71 offset:16\n" \
  "ds_read_b128 v[24:27], v71 offset:32\nds_read_b128 v[28:31], v71 offset:48\nds_read_b128 v[32:35], v72 offset:0\n" \
  "ds_read_b128 v[36:39], v72 offset:16\nds_read_b128 v[40:43], v72 offset:32\nds_read_b128 v[44:47], v72 offset:48\nv_mul_f32 v200, s73, v200\n" \
  "v_mul_f32 v201, s73, v201\nv_mul_f32 v202, s73, v202\nv_mul_f32 v203, s73, v203\nv_mul_f32 v204, s73, v204\nv_mul_f32 v205, s73, v205\n" \
  "v_mul_f32 v206, s73, v206\nv_mul_f32 v207, s73, v207\nv_add_f32 v184, v184, v192\nv_cvt_pk_bf16_f32 v184, v184, 0\ns_waitcnt vmcnt(0)\n" \
  "buffer_store_short v184, v75, %[rq], s72 offen\ns_waitcnt lgkmcnt(10)\nv_mfma_f32_16x16x32_bf16 v[196:199], v[88:91], v[0:3], v[196:199]\n" \
  "v_mfma_f32_16x16x32_bf16 v[196:199], v[92:95], v[4:7], v[196:199]\ns_waitcnt lgkmcnt(8)\n" \
  "v_mfma_f32_16x16x32_bf16 v[196:199], v[96:99], v[8:11], v[196:199]\nv_mfma_f32_16x16x32_bf16 v[196:199], v[100:103], v[12:15], v[196:199]\n" \
  "v_lshlrev_b32 v208, 16, v178\nv_and_b32 v209, 0xffff0000, v178\nv_lshlrev_b32 v210, 16, v179\nv_and_b32 v211, 0xffff0000, v179\ns_waitcnt lgkmcnt(6)\n" \
  "v_mfma_f32_16x16x32_bf16 v[200:203], v[88:91], v[16:19], v[200:203]\nv_mfma_f32_16x16x32_bf16 v[200:203], v[92:95], v[20:23], v[200:203]\n" \
  "s_waitcnt lgkmcnt(4)\nv_mfma_f32_16x16x32_bf16 v[200:203], v[96:99], v[24:27], v[200:203]\n" \
  "v_mfma_f32_16x16x32_bf16 v[200:203], v[100:103], v[28:31], v[200:203]\nv_exp_f32 v220, v196\nv_exp_f32 v221, v197\nv_exp_f32 v222, v198\n" \
  "v_exp_f32 v223, v199\nv_lshlrev_b32 v212, 16, v180\nv_and_b32 v213, 0xffff0000, v180\nv_lshlrev_b32 v214, 16, v181\nv_and_b32 v215, 0xffff0000, v181\n" \
  "s_waitcnt lgkmcnt(2)\nv_mfma_f32_16x16x32_bf16 v[204:207], v[88:91], v[32:35], v[204:207]\n" \
  "v_mfma_f32_16x16x32_bf16 v[204:207], v[92:95], v[36:39], v[204:207]\ns_waitcnt lgkmcnt(0)\n" \
  "v_mfma_f32_16x16x32_bf16 v[204:207], v[96:99], v[40:43], v[204:207]\nv_mfma_f32_16x16x32_bf16 v[204:207], v[100:103], v[44:47], v[204:207]\n" \
  "v_fma_f32 v220, v220, s73, s73\nv_fma_f32 v221, v221, s73, s73\nv_fma_f32 v222, v222, s73, s73\nv_fma_f32 v223, v223, s73, s73\nv_rcp_f32 v220, v220\n" \
  "v_rcp_f32 v221, v221\nv_rcp_f32 v222, v222\nv_rcp_f32 v223, v223\nv_exp_f32 v224, v200\nv_exp_f32 v225, v201\nv_exp_f32 v226, v202\n" \
  "v_exp_f32 v227, v203\nv_lshlrev_b32 v216, 16, v182\nv_and_b32 v217, 0xffff0000, v182\nv_lshlrev_b32 v218, 16, v183\nv_and_b32 v219, 0xffff0000, v183\n" \
  "v_fmac_f32 v208, v196, v220\nv_fmac_f32 v209, v197, v221\nv_fmac_f32 v210, v198, v222\nv_fmac_f32 v211, v199, v223\n" \
  "v_cvt_pk_bf16_f32 v232, v208, v209\nv_cvt_pk_bf16_f32 v233, v210, v211\nds_write_b64 v67, v[232:233]\nv_fma_f32 v224, v224, s73, s73\n" \
  "v_fma_f32 v225, v225, s73, s73\nv_fma_f32 v226, v226, s73, s73\nv_fma_f32 v227, v227, s73, s73\nv_exp_f32 v228, v204\nv_exp_f32 v229, v205\n" \
  "v_exp_f32 v230, v206\nv_exp_f32 v231, v207\nv_rcp_f32 v224, v224\nv_rcp_f32 v225, v225\nv_rcp_f32 v226, v226\nv_rcp_f32 v227, v227\n" \
  "v_fma_f32 v228, v228, s73, s73\nv_fma_f32 v229, v229, s73, s73\nv_fma_f32 v230, v230, s73, s73\nv_fma_f32 v231, v231, s73, s73\nv_rcp_f32 v228, v228\n" \
  "v_rcp_f32 v229, v229\nv_rcp_f32 v230, v230\nv_rcp_f32 v231, v231\nv_fmac_f32 v212, v200, v224\nv_fmac_f32 v213, v201, v225\n" \
  "v_fmac_f32 v214, v202, v226\nv_fmac_f32 v215, v203, v227\nv_cvt_pk_bf16_f32 v234, v212, v213\nv_cvt_pk_bf16_f32 v235, v214, v215\n" \
  "ds_write_b64 v68, v[234:235]\nv_fmac_f32 v216, v204, v228\nv_fmac_f32 v217, v205, v229\nv_fmac_f32 v218, v206, v230\nv_fmac_f32 v219, v207, v231\n" \
  "v_cvt_pk_bf16_f32 v236, v216, v217\nv_cvt_pk_bf16_f32 v237, v218, v219\nds_write_b64 v69, v[236:237]\ns_waitcnt lgkmcnt(0)\ns_barrier\n" \
  "ds_read_b128 v[0:3], v76\ns_cmp_gt_u32 %[w], 1\ns_cbranch_scc1 .Le1%=\nds_read_b128 v[4:7], v76 offset:8192\nv_add_u32 v168, 0x2000, v77\n" \
  "s_waitcnt lgkmcnt(0)\nbuffer_store_dwordx4 v[4:7], v168, %[ro], 0 offen sc1\n.Le1%=:\ns_waitcnt lgkmcnt(0)\n" \
  "buffer_store_dwordx4 v[0:3], v77, %[ro], 0 offen sc1\ns_nop 1\n"
#define EPI_CLOB "v0", "v1", "v2", "v3", "v4", "v5", "v6", "v7", "v8", "v9", "v10", "v11", "v12", "v13", "v14", "v15", "v16", "v17", "v18", "v19", "v20", "v21", "v22", "v23", "v24", "v25", "v26", "v27", "v28", "v29", "v30", "v31", "v32", "v33", "v34", "v35", "v36", "v37", "v38", "v39", "v40", "v41", "v42", "v43", "v44", "v45", "v46", "v47", "v168", "v169", "v170", "v171", "v172", "v173", "v174", "v175", "v176", "v177", "v178", "v179", "v180", "v181", "v182", "v183", "v184", "v185", "v186", "v187", "v188", "v189", "v190", "v191", "v192", "v193", "v194", "v195", "v196", "v197", "v198", "v199", "v200", "v201", "v202", "v203", "v204", "v205", "v206", "v207", "v208", "v209", "v210", "v211", "v212", "v213", "v214", "v215", "v216", "v217", "v218", "v219", "v220", "v221", "v222", "v223", "v224", "v225", "v226", "v227", "v228", "v229", "v230", "v231", "v232", "v233", "v234", "v235", "v236", "v237", "v238", "v239", "s70", "s71", "s72", "s73", "s74", "scc", "memory"

__global__ __launch_bounds__(512, 2) void egnn(const u16* __restrict__ h, const u16* __restrict__ pos, const u16* __restrict__ ea,
                                               const int* __restrict__ src, const int* __restrict__ dst, const u16* __restrict__ we,
                                               const u16* __restrict__ wx, const u16* __restrict__ wh, char* __restrict__ ws,
                                               u16* __restrict__ out) {
  const int g = blockIdx.x, t = threadIdx.x, lane = t & 63, w = __builtin_amdgcn_readfirstlane(t >> 6);
  const int c32 = lane & 31, hh = lane >> 5, c16 = lane & 15, q4 = lane >> 4;
  const int n0 = 39 * g + ((g + 15) >> 4), nn = (g & 15) == 0 ? 40 : 39;
  if (w == 0) {
    unsigned long long pc;
    asm volatile("s_getpc_b64 %0\n\ts_mov_b32 m0, %2\n\ts_mov_b64 exec, 0xff\n\tglobal_load_lds_dword %1, %0\n\ts_mov_b64 exec, -1"
                 : "=&s"(pc) : "v"(((g & 0x78) + (lane & 7)) * 128), "s"(L_DUM) : "memory");
  }
  *LDSP(u32, L_CNT + (t & 255) * 4) = 0;
  u4 dv, sv; u2 ev; u32 d5, s5, e5, epw, w0_, w1_, w2_, w3_, w4_, w5_, w6_, w7_;
  u32 vo = q4 * 256 + ((c16 ^ (2 * q4)) << 4);
  u32 o2 = g * (CH * 2) + t * 8, o1 = g * (CH * 2) + min(t, CH - 1025) * 2, va = vo + 32768 + w * 4096;
  asm volatile("" : "+v"(o2), "+v"(o1), "+v"(va) : "s"(h), "s"(pos), "s"(ea), "s"(src), "s"(dst), "s"(we), "s"(wx), "s"(wh), "s"(ws), "s"(out));
  asm volatile("s_mov_b32 m0, %[m2]\n\ts_nop 0\n\t"
               "global_load_lds_dwordx4 %[a], %[w2]\n\tglobal_load_lds_dwordx4 %[a], %[w2] offset:1024\n\t"
               "global_load_lds_dwordx4 %[b], %[w2] offset:2048\n\tglobal_load_lds_dwordx4 %[b], %[w2] offset:3072\n\tglobal_load_dword %[ep], %[z], %[ws]"
               : [ep] "=&v"(epw)
               : [z] "v"(WS_EP), [ws] "s"(ws), [a] "v"(va), [b] "v"(va ^ 128), [w2] "s"(we), [m2] "s"(L_W2 + w * SBLK)
               : "memory");
  if (w < 4) {
    asm volatile("global_load_dwordx4 %0, %6, %10\n\tglobal_load_dword %3, %8, %10\n\tglobal_load_dwordx4 %1, %6, %11\n\tglobal_load_dword %4, %8, %11\n\t"
                 "global_load_dwordx2 %2, %7, %12\n\tglobal_load_ushort %5, %9, %12 offset:2048"
                 : "=&v"(dv), "=&v"(sv), "=&v"(ev), "=&v"(d5), "=&v"(s5), "=&v"(e5)
                 : "v"(o2 * 2), "v"(o2), "v"(o1 * 2 + 4096), "v"(o1), "s"(dst), "s"(src), "s"(ea) : "memory");
    BAR();
  } else {
    BAR();
    const u32 row = (t >> 4) & 15, hv = n0 * 256 + 4096 + row * 256 + ((c16 ^ row) << 4);
    asm volatile("s_mov_b32 m0, %0\n\ts_nop 0\n\tglobal_load_lds_dwordx4 %1, %2 offset:-4096\n\tglobal_load_lds_dwordx4 %1, %2" ::"s"(L_H + w * 1024), "v"(hv), "s"(h) : "memory");
    if (w < 6) gdma(h, L_H + (w + 4) * 1024, min(hv + 4096, (u32)(NN * 256 - 16)));
  }
  SB();
  if (w >= 6) {
    if (lane < 32 - 16 * (w - 6)) gdma(w == 6 ? we + 32768 : wx, w == 6 ? L_WDA : L_WX, lane * 16);
    if (w == 6)
      asm volatile("global_load_ushort %0, %3, %4\n\tglobal_load_ushort %1, %3, %4 offset:2\n\tglobal_load_ushort %2, %3, %4 offset:4"
                   : "=&v"(w0_), "=&v"(w1_), "=&v"(w2_) : "v"((n0 + min(lane, nn - 1)) * 6), "s"(pos) : "memory");
    else if (g < 64)
      asm volatile("global_load_ushort %0, %8, %9\n\tglobal_load_ushort %4, %8, %9 offset:256\n\t"
                   "global_load_ushort %1, %8, %9 offset:512\n\tglobal_load_ushort %5, %8, %9 offset:768\n\t"
                   "global_load_ushort %2, %8, %9 offset:1024\n\tglobal_load_ushort %6, %8, %9 offset:1280\n\t"
                   "global_load_ushort %3, %8, %9 offset:1536\n\tglobal_load_ushort %7, %8, %9 offset:1792"
                   : "=&v"(w0_), "=&v"(w1_), "=&v"(w2_), "=&v"(w3_), "=&v"(w4_), "=&v"(w5_), "=&v"(w6_), "=&v"(w7_)
                   : "v"((g & 7) * 8192 + (g >> 3) * 32 + q4 * 2048 + c16 * 2), "s"(wh) : "memory");
  }
  const rsrc_t rws = mkrsrc(ws, WS_END);
  u32 tad = L_TAB + lane * 16;
  PIN(tad);
  u32 ra[7];
  if (w < 4) {
    *LDSP(u32, L_WCNT + 4 * min(t, 159)) = t < 8 ? L_S + t * SBLK : 0;
    const u4 zu = {0, 0, 0, 0};
    const u32 za = L_S + w * SBLK + 9216 + lane * 16;
    *LDSP(u4, za) = zu; *LDSP(u4, za + 256) = zu; *LDSP(u4, za + 4 * SBLK) = zu; *LDSP(u4, za + 256 + 4 * SBLK) = zu;
    const u32 pbk = w * 16 + ((t >> 2) & 1) * 8 + ((t >> 3) & 7);
    ra[0] = L_IMG + pbk * 192 + (t & 3) * 16; ra[1] = L_CNT + pbk * 4;
    ra[2] = WS_S1 + ((t >> 3) * NB + g) * 128 + (t & 7) * 16; ra[4] = ra[2] + (WS_S2 - WS_S1) + (t >> 3) * (NB * 128);
    ra[5] = ra[6] = 0;
    ra[3] = (lane >> 2) * 256 + w * 64 + (((lane & 3) ^ (2 * hh)) << 4);
  } else {
#pragma unroll
    for (int rt = 0; rt < 3; ++rt) { const int n = min(16 * rt + c16, NPB - 1); ra[rt] = L_H + n * 256 + ((q4 ^ (n & 15)) << 4); }
    ra[3] = L_W2 + (q4 >> 1) * SBLK + (8 * (q4 & 1) + (c16 >> 2)) * 256 + (c16 & 1) * 8 + (((4 * (w & 3) + ((c16 & 3) >> 1)) ^ (2 * (c16 >> 2) + 8 * (q4 & 1))) << 4);
    ra[4] = L_W2 + c16 * 256 + (((4 * (w & 3) + (q4 >> 1)) ^ (2 * (c16 & 3) + (c16 & 8))) << 4) + (q4 & 1) * 8;
    ra[5] = L_W2 + (t & 255) * 16;
    ra[6] = WS_BT + (n0 + 2 + ((t >> 4) & 15)) * 256 + (((t & 15) ^ (2 * ((t >> 4) & 3) + ((t >> 4) & 8))) << 4);
    const u32 to = min((w - 4) * 4096, 10240);
    asm volatile("s_mov_b32 m0, %0\n\ts_nop 0\n\tglobal_load_lds_dwordx4 %1, %2\n\tglobal_load_lds_dwordx4 %1, %2 offset:1024\n\t"
                 "global_load_lds_dwordx4 %1, %2 offset:2048\n\tglobal_load_lds_dwordx4 %1, %2 offset:3072"
                 ::"s"(L_TAB + to), "v"(to + lane * 16), "s"(&g_tab) : "memory");
  }
#pragma unroll
  for (int i = 0; i < 7; ++i) PIN(ra[i]);
  const u32 v1 = ra[3], wb0 = ra[3], oadr = ra[4], pia = ra[0], pca = ra[1], pga = ra[2], pha = ra[4], poa = ra[5], pba = ra[6];
  const u32* hadr = ra;
  const f4 z4 = {0.f, 0.f, 0.f, 0.f};
  if (w < 4) WAITV(0);
  asm volatile("s_waitcnt vmcnt(4)" : "+v"(dv), "+v"(sv), "+v"(ev), "+v"(d5), "+v"(s5), "+v"(e5), "+v"(epw), "+v"(w0_), "+v"(w1_), "+v"(w2_), "+v"(w3_),
               "+v"(w4_), "+v"(w5_), "+v"(w6_), "+v"(w7_)::"memory");
  const u32 epoch = __builtin_amdgcn_readfirstlane(epw) + 0x9E3779B1u, ep8 = (epoch * 0x85EBCA6Bu) >> 24;
  if (w < 4) {
    const u32 dd[5] = {dv[0], dv[1], dv[2], dv[3], d5}, ss[5] = {sv[0], sv[1], sv[2], sv[3], s5};
    const u32 ee[5] = {ev[0] << 16, ev[0] & 0xffff0000u, ev[1] << 16, ev[1] & 0xffff0000u, e5 << 16};
    u32 pr[5], ca[5], rk[5]; u2 en[5];
    u32 five = 156;
    asm volatile("" : "+s"(five));
#pragma unroll
    for (int i = 0; i < 5; ++i) {
      pr[i] = __umul24(dd[i], 429497u); ca[i] = (pr[i] >> 22) & ~3u;
      if (i == 4) ca[i] = t < CH - 1024 ? ca[i] : 1024u;
      rk[i] = ldsadd(L_CNT + ca[i]);
    }
#define W1HALF(h) asm volatile("s_mov_b32 m0, %[m]\n\ts_movk_i32 s69, 3072\n\tbuffer_load_dwordx4 %[a], %[r], 0 offen lds\n\ts_movk_i32 s70, 6144\n\t" \
                 "buffer_load_dwordx4 %[a], %[r], s69 offen offset:1024 lds\n\ts_movk_i32 s71, 9216\n\t" \
                 "buffer_load_dwordx4 %[a], %[r], s70 offen offset:2048 lds\n\tbuffer_load_dwordx4 %[a], %[r], s71 offen offset:3072 lds" \
                 :: [a] "v"(v1 + h * 16384), [r] "s"(mkrsrc(we, 32768)), [m] "s"(L_W1 + (2 * w + h) * SBLK) : "s69", "s70", "s71", "memory")
    SB();
#pragma unroll
    for (int i = 0; i < 5; ++i) {
      en[i][0] = ss[i] + ((epoch << 16) + 2);
      en[i][1] = ee[i] | (mulhi24(pr[i], 10000u) * 32 + ep8 * 256 - __umul24(mulhi24(pr[i], 2000u), five));
      PIN(en[i]);
    }
#pragma unroll
    for (int i = 0; i < 5; ++i) *LDSP(u2, L_IMG + __umul24(ca[i], 48u) + min(rk[i], 23u) * 8) = en[i];
    SB();
    W1HALF(0); W1HALF(1);
    BAR();
    u4 e[4], f[4]; u32 c[4], m[4];
#pragma unroll
    for (int i = 0; i < 4; ++i) { e[i] = *LDSP(u4, pia + i * 12288); c[i] = *LDSP(u32, pca + i * 256); }
#pragma unroll
    for (int i = 0; i < 4; ++i) f[i] = *LDSP(u4, pia + i * 12288 + 64);
#pragma unroll
    for (int i = 0; i < 4; ++i) {
      e[i][0] = __builtin_amdgcn_perm(epoch, e[i][0], 0x05040100u); e[i][2] = __builtin_amdgcn_perm(epoch, e[i][2], 0x07060100u);
      e[i][1] = __builtin_amdgcn_perm(min(c[i], 255u), e[i][1], 0x03020400u); e[i][3] = __builtin_amdgcn_perm(ep8, e[i][3], 0x03020400u);
      __builtin_amdgcn_raw_buffer_store_b128(e[i], rws, pga + i * (32 * NB * 128), 0, 16);
      m[i] = max(c[i], (u32)__builtin_amdgcn_mov_dpp(c[i], 0x141, 0xf, 0xf, true));
    }
#pragma unroll
    for (int i = 0; i < 4; ++i)
      asm volatile("v_cmp_lt_u32 vcc, 8, %[c]\n\ts_mov_b64 exec, vcc\n\tbuffer_store_dwordx4 %[f], %[a], %[r], %[s] offen sc1\n\ts_mov_b64 exec, -1\n\ts_nop 0"
                   :: [c] "v"(m[i]), [f] "v"(f[i]), [a] "v"(pha), [r] "s"(rws), [s] "s"(i * (32 * NB * 256)) : "vcc", "memory");
    if (__builtin_amdgcn_ballot_w64(max(max(m[0], m[1]), max(m[2], m[3])) > 16) != 0) {
#pragma unroll
      for (int i = 0; i < 4; ++i)
        __builtin_amdgcn_raw_buffer_store_b128(*LDSP(u4, pia + i * 12288 + 128), rws, m[i] > 16 ? pha + (NB * 128 + i * (32 * NB * 256)) : OOB, 0, 16);
    }
  } else {
    BAR();
    __builtin_amdgcn_s_setprio(1);
    s8 f0[4], f1[4], hf[3][4];
#pragma unroll
    for (int ks = 0; ks < 4; ++ks) {
      f0[ks] = trfrag(wb0 + ks * 2 * SBLK, wb0 + ks * 2 * SBLK + 1024); f1[ks] = trfrag((wb0 ^ 32) + ks * 2 * SBLK, (wb0 ^ 32) + ks * 2 * SBLK + 1024);
      hf[0][ks] = *LDSP(s8, hadr[0] ^ (ks * 64));
    }
#pragma unroll
    for (int rt = 1; rt < 3; ++rt)
#pragma unroll
      for (int ks = 0; ks < 4; ++ks) hf[rt][ks] = *LDSP(s8, hadr[rt] ^ (ks * 64));
    SB();
#pragma unroll
    for (int rt = 0; rt < 3; ++rt) {
      f4 a0 = z4, a1 = z4;
#pragma unroll
      for (int ks = 0; ks < 4; ++ks) { a0 = MF16(f0[ks], hf[rt][ks], a0); a1 = MF16(f1[ks], hf[rt][ks], a1); }
      a0 = a0 * NLOG2E; a1 = a1 * NLOG2E;
      *LDSP(u2, oadr + rt * SBLK) = u2{pk2(a0[0], a0[1]), pk2(a0[2], a0[3])};
      *LDSP(u2, (oadr ^ 32) + rt * SBLK) = u2{pk2(a1[0], a1[1]), pk2(a1[2], a1[3])};
    }
    WAITV(0);
  }
  BAR();
  const u4 aad = *LDSP(u4, tad + 12288), ac = *LDSP(u4, tad + 13312);
  if (w >= 4) {
    const u4 b0 = *LDSP(u4, poa), b1 = *LDSP(u4, poa + SBLK);
    __builtin_amdgcn_raw_buffer_store_b128(b0, rws, pba, 0, 16);
    __builtin_amdgcn_raw_buffer_store_b128(b1, rws, pba + 4096, 0, 16);
    if (w < 6) __builtin_amdgcn_raw_buffer_store_b128(*LDSP(u4, poa + 2 * SBLK), rws, (t >> 4) + 16 < nn ? pba + 8192 : OOB, 0, 16);
    else if (w == 6) { if (lane < NPB) *LDSP(f4, L_PD + lane * 16) = f4{bflo(w0_), bflo(w1_), bflo(w2_), 0.f}; }
    else if (g <= 64) {
      const bool z = g == 64;
      __builtin_amdgcn_raw_buffer_store_b128(z ? u4{0, 0, 0, 0} : u4{w0_ | (w4_ << 16), w1_ | (w5_ << 16), w2_ | (w6_ << 16), w3_ | (w7_ << 16)}, rws,
                                             z && lane >= 32 ? OOB : lane * 16 + (WS_WT + g * 1024), 0, 16);
    }
  }
  SB();
  const u32 stg = w * STG;
  u16v kc, tc, ec;
  u32 wxr[4];
  auto rdtab = [&]() {
#pragma unroll
  for (int i = 0; i < 4; ++i) {
    const u4 a = *LDSP(u4, tad + i * 1024), b = *LDSP(u4, tad + 4096 + i * 1024), c = *LDSP(u4, tad + 8192 + i * 1024);
#pragma unroll
    for (int j = 0; j < 4; ++j) { kc[4 * i + j] = a[j]; tc[4 * i + j] = b[j]; ec[4 * i + j] = c[j]; }
  }
#pragma unroll
  for (int ct = 0; ct < 4; ++ct) wxr[ct] = *LDSP(u16, ac[3] + ct * 64);
  };
  f16v hl[2];
  if (w < 4) {
    const u32 wb = ac[0] + 2 * w * SBLK;
    s8 f[2][4], hf[4][4];
    u32 wd[2], wa[2];
#pragma unroll
    for (int c = 0; c < 2; ++c) { wd[c] = *LDSP(u16, ac[1] + w * 64 + c * 32); wa[c] = *LDSP(u16, ac[1] + w * 64 + c * 32 + 256); }
#pragma unroll
    for (int ks = 0; ks < 4; ++ks) hf[0][ks] = *LDSP(s8, aad[0] ^ (ks * 64));
    WAITV(8);
#pragma unroll
    for (int ks = 0; ks < 4; ++ks) {
      const int o = (ks >> 1) * SBLK + (ks & 1) * 2048;
      f[0][ks] = trfrag(wb + o, wb + o + 256); f[1][ks] = trfrag((wb ^ 32) + o, (wb ^ 32) + o + 256);
    }
#pragma unroll
    for (int rt = 1; rt < 4; ++rt)
#pragma unroll
      for (int ks = 0; ks < 4; ++ks) hf[rt][ks] = *LDSP(s8, aad[rt] ^ (ks * 64));
    SB();
    f4 c0[2];
#pragma unroll
    for (int c = 0; c < 2; ++c) {
      const float x = bflo(wd[c] & ~ac[2]) * NLOG2E;
      c0[c] = f4{bflo(wd[c] & ac[2]), bflo(wa[c] & ac[2]), 0.f, (x - bflo(pk2(x, 0.f))) * NLN2};
    }
#pragma unroll
    for (int rt = 0; rt < 4; ++rt) {
      f4 a[2];
#pragma unroll
      for (int c = 0; c < 2; ++c) a[c] = c0[c];
#pragma unroll
      for (int ks = 0; ks < 4; ++ks)
#pragma unroll
        for (int c = 0; c < 2; ++c) a[c] = MF16(hf[rt][ks], f[c][ks], a[c]);
#pragma unroll
      for (int j = 0; j < 8; ++j) hl[rt >> 1][(rt & 1) * 8 + j] = a[j >> 2][j & 3];
      if (rt == 1) { SB(); rdtab(); SB(); }
    }
  } else {
    asm volatile("" : "=v"(hl[0]), "=v"(hl[1]));
    rdtab();
  }
  kc[8] += stg; kc[9] += stg;
  const u32 gp = (g >> 4) * 8 + (g & 7), gw = (g & 8) * 8 + w * 2048;
  tc[0] += gp * (NB * 128) + gw; tc[1] += gp * (NB * 256) + gw; tc[12] += w * (16 * FLS); tc[14] += (w & 2) * 512 + (w & 1) * 8;
  ec[3] ^= w * 32; ec[4] ^= w * 32; ec[5] ^= w * 32; ec[12] += w * 1024;
  ec[13] = (ec[13] ^ ((w & 3) << 6)) + w * 1024;
  PIN(ec);
  f4 wv;
#pragma unroll
  for (int ct = 0; ct < 4; ++ct) wv[ct] = bflo(wxr[ct]) * NLN2;
  u16v wa_, wb_;
  int win, rem; u32 st;
  asm volatile(CONS_ASM
               : "=&{v[104:119]}"(wa_), "=&{v[88:103]}"(wb_), [win] "=&s"(win), [rem] "=&s"(rem), [st] "=&s"(st),
                 "+{v[136:151]}"(kc), "+{v[218:233]}"(tc), "+{v[166:181]}"(hl[0]), "+{v[182:197]}"(hl[1])
               : [stg] "s"(stg), [rt] "s"(w < 4 ? w : 11 - w), [ep] "s"(epoch), [e8] "s"(ep8), [go] "s"(WS_FL + g * FLS), [rw] "s"(rws),
                 [rb] "s"(mkrsrc(ws + WS_BT - 4096, (NN + 2) * 256 + 4096)), [rp] "s"(mkrsrc(pos, NN * 6)), [fp] "s"(WS_WT + w * 8192),
                 "{v[64:79]}"(ec), "{v[80:83]}"(wv)
               : CONS_CLOB);
  win = __builtin_amdgcn_readfirstlane(win); st = __builtin_amdgcn_readfirstlane(st);
  if (__builtin_expect(st != 0, 0)) {
    const int lane = ec[15] >> 2, t = w * 64 + lane, hh = lane >> 5, c16 = lane & 15, q4 = lane >> 4;
    const u32 sblk = L_S + win * SBLK, mblk = sblk + ((win * 144) & 255) + mplane((lane >> 3) & 3) + hh * 16 + (lane & 7) * 2;
    float pacc[9];
    const int nb = n0 + 5 * win, nc = min(5, nn - 5 * win);
    f2 av[5], acc[5][4];
    auto rowdot = [&](int n, const u16* W) {
      f2 a = {0.f, 0.f};
#pragma unroll 1
      for (int k = 0; k < 128; ++k) { const float x = bflo(h[n * 128 + k]); a.x += x * bflo(W[k * 128 + lane]); a.y += x * bflo(W[k * 128 + 64 + lane]); }
      return a;
    };
#pragma unroll
    for (int i = 0; i < 5; ++i) {
      av[i] = rowdot(min(nb + i, NN - 1), we);
#pragma unroll
      for (int k = 0; k < 4; ++k) acc[i][k] = f2{0.f, 0.f};
    }
    const f2 wdv = {bflo(we[256 * 128 + lane]), bflo(we[256 * 128 + 64 + lane])}, wav = {bflo(we[257 * 128 + lane]), bflo(we[257 * 128 + 64 + lane])};
#pragma unroll 1
    for (int eb = 0; eb < EE; eb += 64) {
      const int dl = dst[eb + lane] - nb;
      unsigned long long mk = __builtin_amdgcn_ballot_w64((unsigned)dl < (unsigned)nc);
      while (mk) {
        const int l = __builtin_ctzll(mk); mk &= mk - 1;
        const int e = eb + l, nw = __builtin_amdgcn_readlane(dl, l), s = src[e], d = nb + nw;
        u32 w2, dz, d2;
        geom(bflo(pos[3 * d]), bflo(pos[3 * d + 1]), bflo(pos[3 * d + 2]), pos[3 * s] | ((u32)pos[3 * s + 1] << 16), pos[3 * s + 2], w2, dz, d2);
        const f2 tb = rowdot(s, we + 128 * 128) + bflo(d2) * wdv + bflo(ea[e]) * wav;
#pragma unroll
        for (int i = 0; i < 5; ++i)
          if (nw == i) {
            const f2 x = av[i] + tb;
            f2 m;
            m.x = x.x * __builtin_amdgcn_rcpf(1.0f + __builtin_amdgcn_exp2f(x.x * NLOG2E));
            m.y = x.y * __builtin_amdgcn_rcpf(1.0f + __builtin_amdgcn_exp2f(x.y * NLOG2E));
            acc[i][0] += m; acc[i][1] += m * bflo(w2); acc[i][2] += m * bfhi(w2); acc[i][3] += m * bflo(dz);
          }
      }
    }
    const f2 wxs = {bflo(wx[lane]), bflo(wx[64 + lane])};
#pragma unroll
    for (int i = 0; i < 9; ++i) pacc[i] = 0.f;
#pragma unroll
    for (int i = 0; i < 5; ++i) {
      const u32 m = pk2(acc[i][0].x * NLOG2E, acc[i][0].y * NLOG2E);
      *LDSP(u16, mblk + i * 80) = (u16)m; *LDSP(u16, mblk + i * 80 + 32) = (u16)(m >> 16);
#pragma unroll
      for (int k = 1; k < 4; ++k) {
        float v = acc[i][k].x * wxs.x + acc[i][k].y * wxs.y;
        v += __shfl_xor(v, 32);
        if (hh == (i & 1)) pacc[(i >> 1) * 3 + k - 1] = v;
      }
    }
#pragma unroll
    for (int i = 0; i < 16; ++i) {
      const u16* p = wh + (32 * (i >> 2) + 8 * q4 + 2 * (i & 3)) * 128 + 16 * w + c16;
      wa_[i] = p[0] | ((u32)p[128] << 16); wb_[i] = p[16384] | ((u32)p[16384 + 128] << 16);
    }
#pragma unroll
    for (int i = 0; i < 9; ++i) *LDSP(float, sblk + PART + i * 256 + lane * 4) = pacc[i];
    if (t == 0 && (g == 0 || (st & 2))) __hip_atomic_store((gu32*)(ws + WS_EP), (st & 2) ? epoch * 0x2545F491u + g : epoch, __ATOMIC_RELAXED, __HIP_MEMORY_SCOPE_AGENT);
  }
  asm volatile(EPI_ASM
               : "+{v[64:79]}"(ec)
               : [win] "s"(win), [w] "s"(w), [ro] "s"(mkrsrc(out + n0 * 128, nn * 256)), [rq] "s"(mkrsrc(out + NN * 128 + n0 * 3, nn * 6)), "{v[104:119]}"(wa_), "{v[88:103]}"(wb_)
               : EPI_CLOB);
}

extern "C" void kernel_launch(void* const* d_in, const int* in_sizes, int n_in, void* d_out, int out_size, void* d_ws, size_t ws_size,
                              hipStream_t stream) {
  (void)hipFuncSetAttribute((const void*)egnn, hipFuncAttributeMaxDynamicSharedMemorySize, L_END);
  egnn<<<NG, 512, L_END, stream>>>((const u16*)d_in[0], (const u16*)d_in[1], (const u16*)d_in[2], (const int*)d_in[3], (const int*)d_in[4],
                                   (const u16*)d_in[5], (const u16*)d_in[6], (const u16*)d_in[7], (char*)d_ws, (u16*)d_out);
}
```
